```python
import torch

def _softmax(x, axis=-1):
    return torch.softmax(x, dim=axis)
def _gelu(x):
    return torch.nn.functional.gelu(x, approximate="tanh")
def _silu(x):
    return torch.nn.functional.silu(x)
def _sigmoid(x):
    return torch.sigmoid(x)
def _layernorm(x, axis=-1, eps=1e-5):
    m = x.mean(dim=axis, keepdim=True)
    v = x.var(dim=axis, keepdim=True, unbiased=False)
    return (x - m) / torch.sqrt(v + eps)
def _scatter_add(tgt, idx, src, axis=0):
    return tgt.clone().index_add_(axis, idx, src)
def _segment_sum(src, seg, n):
    out = torch.zeros((n,) + src.shape[1:], dtype=src.dtype)
    return out.index_add_(0, seg, src)
def _pad(x, pw):
    flat = tuple(v for pair in reversed(pw) for v in pair)
    return torch.nn.functional.pad(x, flat)

kernel_name = 'parallel_sum__spectral_gfnet__glu_gated_bilinear__prompt'

def setup_inputs():

    _w = (lambda a: (lambda t: t.to(torch.bfloat16) if t.is_floating_point() else t)(torch.as_tensor(a)))
    def _A_setup():
        B, S, D = 1, 2048, 640
        Df = D // 2 + 1
        return {
            "x": _w((lambda *s: torch.randn(*s, dtype=torch.bfloat16) * 0.05)(B, S, D)),
            "w_r": _w((lambda *s: torch.randn(*s, dtype=torch.bfloat16) * 0.4)(S, Df) * 0.02),
            "w_i": _w((lambda *s: torch.randn(*s, dtype=torch.bfloat16) * 0.4)(S, Df) * 0.02),
        }
    _A_in = _A_setup()

    def _B_setup():
        B, S, D, F = 1, 2048, 640, 1536
        return {
            "x": _w((lambda *s: torch.randn(*s, dtype=torch.bfloat16) * 0.05)(B, S, D)),
            "w_gate": _w((lambda *s: torch.randn(*s, dtype=torch.bfloat16) * 0.2)(D, F) * (D ** -0.5)),
            "w_up": _w((lambda *s: torch.randn(*s, dtype=torch.bfloat16) * 0.2)(D, F) * (D ** -0.5)),
            "w_down": _w((lambda *s: torch.randn(*s, dtype=torch.bfloat16) * 0.2)(F, D) * (F ** -0.5)),
        }
    _B_in = _B_setup()

    return {
        "A_x": _A_in["x"],
        "A_w_r": _A_in["w_r"],
        "A_w_i": _A_in["w_i"],
        "B_x": _B_in["x"],
        "B_w_gate": _B_in["w_gate"],
        "B_w_up": _B_in["w_up"],
        "B_w_down": _B_in["w_down"],
    }

def reference(A_x, A_w_r, A_w_i, B_x, B_w_gate, B_w_up, B_w_down):
    def _A_ref(x, w_r, w_i):
        B, S, D = x.shape
        X = (lambda x, axes=(-2,-1): torch.fft.rfft2(x.float(), dim=axes))(x, axes=(-2, -1))
        W = w_r + 1j * w_i
        X = X * W
        y = (lambda x, s, axes=(-2,-1): torch.fft.irfft2(x, s=s, dim=axes).to(torch.bfloat16))(X, (S, D), axes=(-2, -1))
        return y

    def _B_ref(x, w_gate, w_up, w_down):
        g = torch.einsum("bsd,df->bsf", x, w_gate)
        u = torch.einsum("bsd,df->bsf", x, w_up)
        h = g * u
        return torch.einsum("bsf,fd->bsd", h, w_down)

    x = A_x
    xn = _layernorm(x, axis=-1)
    return x + _A_ref(xn, A_w_r, A_w_i) + _B_ref(xn, B_w_gate, B_w_up, B_w_down)
```

```cpp
#include <hip/hip_runtime.h>
#include <hip/hip_bf16.h>
#include <stdint.h>
typedef unsigned short u16;
constexpr int S=2048, D=640, DF=321, F=1536;
__device__ __forceinline__ float bf2f(u16 v){ return __uint_as_float(((unsigned)v)<<16); }
__device__ __forceinline__ u16 f2bf(float f){ unsigned u=__float_as_uint(f); u += 0x7fffu + ((u>>16)&1u); return (u16)(u>>16);}
__device__ __forceinline__ float rbf(float f){ return bf2f(f2bf(f)); }

__global__ void k_ln(const u16* x, float* xn){
  int s=blockIdx.x; int t=threadIdx.x;
  float v[10]; float sum=0;
  for(int i=0;i<10;i++){ v[i]=bf2f(x[s*D+t+64*i]); sum+=v[i]; }
  for(int o=32;o;o>>=1) sum+=__shfl_xor(sum,o);
  float mean=sum/D; float sq=0;
  for(int i=0;i<10;i++){ float d=v[i]-mean; sq+=d*d; }
  for(int o=32;o;o>>=1) sq+=__shfl_xor(sq,o);
  float m=rbf(mean), var=rbf(sq/D);
  float sd=rbf(sqrtf(rbf(var+1e-5f)));
  for(int i=0;i<10;i++){ xn[s*D+t+64*i]=rbf(rbf(v[i]-m)/sd); }
}
__global__ void k_dft_d(const float* xn, float2* Z){
  __shared__ float row[D]; __shared__ float2 tw[D];
  int s=blockIdx.x;
  for(int i=threadIdx.x;i<D;i+=blockDim.x){ row[i]=xn[s*D+i]; float sn,cs; sincospif(-2.0f*i/D,&sn,&cs); tw[i]=make_float2(cs,sn);}
  __syncthreads();
  for(int k=threadIdx.x;k<DF;k+=blockDim.x){ float re=0,im=0; int idx=0; for(int d=0;d<D;d++){ re+=row[d]*tw[idx].x; im+=row[d]*tw[idx].y; idx+=k; if(idx>=D) idx-=D;} Z[k*S+s]=make_float2(re,im);}
}
__global__ void k_col(float2* Z, const u16* wr, const u16* wi){
  __shared__ float2 a[S]; __shared__ float2 b[S]; __shared__ float2 tw[S];
  int k=blockIdx.x;
  for(int i=threadIdx.x;i<S;i+=blockDim.x){ a[i]=Z[k*S+i]; float sn,cs; sincospif(-2.0f*i/S,&sn,&cs); tw[i]=make_float2(cs,sn);}
  __syncthreads();
  for(int f=threadIdx.x;f<S;f+=blockDim.x){ float re=0,im=0; for(int s=0;s<S;s++){ float2 w=tw[(s*f)&(S-1)]; float2 z=a[s]; re+=z.x*w.x-z.y*w.y; im+=z.x*w.y+z.y*w.x;}
    float r=bf2f(wr[f*DF+k]), q=bf2f(wi[f*DF+k]); b[f]=make_float2(re*r-im*q, re*q+im*r);}
  __syncthreads();
  for(int s=threadIdx.x;s<S;s+=blockDim.x){ float re=0,im=0; for(int f=0;f<S;f++){ float2 w=tw[(s*f)&(S-1)]; float2 z=b[f]; re+=z.x*w.x+z.y*w.y; im+=-z.x*w.y+z.y*w.x;} Z[k*S+s]=make_float2(re,im);}
}
__global__ void k_glu1(const float* xn, const u16* wg, const u16* wu, float* h){
  __shared__ float row[D]; int s=blockIdx.x;
  for(int i=threadIdx.x;i<D;i+=blockDim.x) row[i]=xn[s*D+i];
  __syncthreads();
  for(int f=threadIdx.x;f<F;f+=blockDim.x){ float g=0,u=0; for(int d=0;d<D;d++){ g+=row[d]*bf2f(wg[d*F+f]); u+=row[d]*bf2f(wu[d*F+f]); } h[s*F+f]=rbf(rbf(g)*rbf(u)); }
}
__global__ void k_fin(const u16* x, const float2* Z, const float* h, const u16* wd, u16* out){
  __shared__ float2 z[DF]; __shared__ float hr[F]; __shared__ float2 tw[D];
  int s=blockIdx.x;
  for(int i=threadIdx.x;i<DF;i+=blockDim.x) z[i]=Z[i*S+s];
  for(int i=threadIdx.x;i<F;i+=blockDim.x) hr[i]=h[s*F+i];
  for(int i=threadIdx.x;i<D;i+=blockDim.x){ float sn,cs; sincospif(2.0f*i/D,&sn,&cs); tw[i]=make_float2(cs,sn);}
  __syncthreads();
  for(int d=threadIdx.x;d<D;d+=blockDim.x){
    float acc=0; int idx=d; for(int k=1;k<320;k++){ acc+=z[k].x*tw[idx].x - z[k].y*tw[idx].y; idx+=d; if(idx>=D) idx-=D; }
    acc=2*acc+z[0].x+((d&1)?-z[320].x:z[320].x);
    acc*=1.0f/(S*D);
    float b=0; for(int f=0;f<F;f++) b+=hr[f]*bf2f(wd[f*D+d]);
    out[s*D+d]=f2bf(rbf(bf2f(x[s*D+d])+rbf(acc))+rbf(b));
  }
}
extern "C" void kernel_launch(void* const* d_in, const int* in_sizes, int n_in, void* d_out, int out_size, void* d_ws, size_t ws_size, hipStream_t stream){
  const u16* x=(const u16*)d_in[0]; const u16* wr=(const u16*)d_in[1]; const u16* wi=(const u16*)d_in[2];
  const u16* wg=(const u16*)d_in[4]; const u16* wu=(const u16*)d_in[5]; const u16* wd=(const u16*)d_in[6];
  char* ws=(char*)d_ws; float* xn=(float*)ws; float2* Z=(float2*)(ws+(8<<20)); float* h=(float*)(ws+(16<<20));
  k_ln<<<S,64,0,stream>>>(x,xn);
  k_dft_d<<<S,320,0,stream>>>(xn,Z);
  k_col<<<DF,1024,0,stream>>>(Z,wr,wi);
  k_glu1<<<S,512,0,stream>>>(xn,wg,wu,h);
  k_fin<<<S,640,0,stream>>>(x,Z,h,wd,(u16*)d_out);
}
```

```cpp
#include <hip/hip_runtime.h>
#include <utility>

typedef unsigned u32;
typedef unsigned short u16;
typedef unsigned long long u64;
typedef unsigned v4u __attribute__((ext_vector_type(4)));
typedef unsigned v2u __attribute__((ext_vector_type(2)));
typedef float f32x2 __attribute__((ext_vector_type(2)));
typedef float f32x4 __attribute__((ext_vector_type(4)));
typedef float f32x16 __attribute__((ext_vector_type(16)));
typedef __bf16 bf16x2 __attribute__((ext_vector_type(2)));
typedef __bf16 bf16x4 __attribute__((ext_vector_type(4)));
typedef __bf16 bf16x8 __attribute__((ext_vector_type(8)));
typedef __amdgpu_buffer_rsrc_t rsrc_t;
#define LAS __attribute__((address_space(3)))
#define DI __device__ __forceinline__
#define GAP() __builtin_amdgcn_sched_barrier(0)
#define IC(x) std::integral_constant<int, (x)>{}
#pragma clang diagnostic ignored "-Wint-to-pointer-cast"

#define FUSED 0

template <class F, int... Is> DI void sfor_(F&& f, std::integer_sequence<int, Is...>) { (f(std::integral_constant<int, Is>{}), ...); }
template <int N, class F> DI void sfor(F&& f) { sfor_(f, std::make_integer_sequence<int, N>{}); }

constexpr u32 WS_FZ = 0x0400, WS_FW = 0x0800, WS_FH = 0x0C00, WS_FY = 0x1000;
constexpr u32 WS_STAT = 0x4000;
constexpr u32 WS_CS = 0x8000;
constexpr u32 WS_Z = 0x40000;
constexpr u32 WS_Y = WS_Z + 0x280000;
constexpr u32 WS_WT = WS_Y + 0x280000;
constexpr u32 WS_WD = WS_WT + 0x290000;
constexpr u32 WS_H = WS_WD + 0x1E0000;
constexpr u32 WS_END = WS_H + 0x600000;
constexpr u32 OOB = 0x40000000u;
constexpr int SC1 = 16;

DI u32 cvtpk(float lo, float hi) { f32x2 v = {lo, hi}; return __builtin_bit_cast(u32, __builtin_convertvector(v, bf16x2)); }
DI float bflo(u32 v) { return __uint_as_float(v << 16); }
DI float bfhi(u32 v) { return __uint_as_float(v & 0xffff0000u); }
DI float rbf(float f) { return bflo(cvtpk(f, f)); }
DI bf16x8 asbf(v4u v) { return __builtin_bit_cast(bf16x8, v); }
DI rsrc_t mkrs(const void* p, u32 n) { return __builtin_amdgcn_make_buffer_rsrc((void*)p, 0, n, 0x00020000); }
DI void DMA(u32 m0v, u32 voff, rsrc_t rs, u32 soff) {
  asm volatile("s_mov_b32 m0, %0\n\ts_nop 0\n\tbuffer_load_dwordx4 %1, %2, %3 offen lds" ::"s"(m0v), "v"(voff), "s"(rs), "s"(soff) : "memory");
}
template <int N> DI void WAITBAR() { asm volatile("s_waitcnt vmcnt(%0) lgkmcnt(0)\n\ts_barrier" ::"i"(N) : "memory"); }
template <int N> DI void WAITBAR_V() { asm volatile("s_waitcnt vmcnt(%0)\n\ts_barrier" ::"i"(N) : "memory"); }
DI void DRAIN() { asm volatile("s_waitcnt vmcnt(0)" ::: "memory"); }
DI void OPAQUE(u32& v) { asm volatile("" : "+v"(v)); }
DI void CFENCE() { asm volatile("" ::: "memory"); }
DI bf16x4 trr(u32 a) { return __builtin_amdgcn_ds_read_tr16_b64_v4bf16((LAS bf16x4*)a); }
DI bf16x8 cat(bf16x4 a, bf16x4 b) { return __builtin_shufflevector(a, b, 0, 1, 2, 3, 4, 5, 6, 7); }
DI f32x16 mfma32(bf16x8 a, bf16x8 b, f32x16 c) { return __builtin_amdgcn_mfma_f32_32x32x16_bf16(a, b, c, 0, 0, 0); }
DI f32x4 mfma16(bf16x8 a, bf16x8 b, f32x4 c) { return __builtin_amdgcn_mfma_f32_16x16x32_bf16(a, b, c, 0, 0, 0); }
template <int AUX = 0> DI v4u ld128(rsrc_t rs, u32 voff, u32 soff = 0) { return __builtin_amdgcn_raw_buffer_load_b128(rs, voff, soff, AUX); }
template <int AUX = 0> DI v2u ld64(rsrc_t rs, u32 voff, u32 soff = 0) { return __builtin_amdgcn_raw_buffer_load_b64(rs, voff, soff, AUX); }
template <int AUX = 0> DI u32 ld32(rsrc_t rs, u32 voff, u32 soff = 0) { return __builtin_amdgcn_raw_buffer_load_b32(rs, voff, soff, AUX); }
template <int AUX = 0> DI void st128(v4u v, rsrc_t rs, u32 voff, u32 soff = 0) { __builtin_amdgcn_raw_buffer_store_b128(v, rs, voff, soff, AUX); }
template <int AUX = 0> DI void st64(v2u v, rsrc_t rs, u32 voff, u32 soff = 0) { __builtin_amdgcn_raw_buffer_store_b64(v, rs, voff, soff, AUX); }
template <int AUX = 0> DI void st32(u32 v, rsrc_t rs, u32 voff, u32 soff = 0) { __builtin_amdgcn_raw_buffer_store_b32(v, rs, voff, soff, AUX); }
DI float cosr(float rev) { return __builtin_amdgcn_cosf(rev); }
DI float sinr(float rev) { return __builtin_amdgcn_sinf(rev); }
DI float wave_sum(float v) {
#pragma unroll
  for (int o = 32; o; o >>= 1) v += __shfl_xor(v, o);
  return v;
}

struct Ctx {
  rsrc_t rx, rws;
  const u16 *wr, *wi, *wg, *wu, *wd;
  u16* out;
  u32 lb;
  u32 tag;
  int l, w, xcd, idx;
};

struct Pre { v4u a, b, c0, c1; };
DI void pre_issue(const Ctx& c, Pre& p) {
  const int l = c.l, qd = c.xcd >> 1;
  const u32 row = (c.xcd & 1) * 1024 + qd * 256 + c.idx * 8 + c.w;
  p.a = ld128(c.rx, row * 1280 + l * 16);
  p.b = ld128(c.rx, l < 16 ? row * 1280 + 1024 + l * 16 : OOB);
  const int j = (c.xcd & 1) * 32 + c.idx, chunk = j % 12, part = j / 12;
  const rsrc_t rw = mkrs(chunk >= 6 ? c.wu : c.wg, 640 * 3072);
  const u32 vo = j < 60 ? (u32)((128 * part + 16 * c.w + (l >> 3)) * 3072 + (384 * qd + 64 * (chunk % 6)) * 2 + (l & 7) * 16) : OOB;
  p.c0 = ld128(rw, vo);
  p.c1 = ld128(rw, vo, 8 * 3072);
}
DI void pre_compute(const Ctx& c, const Pre& p, u32 scratch) {
  const int l = c.l, qd = c.xcd >> 1;
  const u32 row = (c.xcd & 1) * 1024 + qd * 256 + c.idx * 8 + c.w;
  float v[16];
#pragma unroll
  for (int e = 0; e < 4; ++e) { v[2 * e] = bflo(p.a[e]); v[2 * e + 1] = bfhi(p.a[e]); v[8 + 2 * e] = bflo(p.b[e]); v[9 + 2 * e] = bfhi(p.b[e]); }
  float s = 0.f;
#pragma unroll
  for (int e = 0; e < 16; ++e) s += v[e];
  const float mean = wave_sum(s) * (1.0f / 640.0f);
  float q0 = 0.f, q1 = 0.f;
#pragma unroll
  for (int e = 0; e < 8; ++e) { const float d0 = v[e] - mean, d1 = v[8 + e] - mean; q0 += d0 * d0; q1 += d1 * d1; }
  const float var = wave_sum(q0 + (l < 16 ? q1 : 0.f)) * (1.0f / 640.0f);
  const float sd = sqrtf(rbf(rbf(var) + 1e-5f));
  if (l == 0) st64<SC1>(v2u{cvtpk(mean, sd), c.tag}, c.rws, WS_STAT + row * 8);
  float t[8];
#pragma unroll
  for (int e = 0; e < 4; ++e) { t[2 * e] = bflo(p.c0[e]) + bflo(p.c1[e]); t[2 * e + 1] = bfhi(p.c0[e]) + bfhi(p.c1[e]); }
#pragma unroll
  for (int e = 0; e < 8; ++e) { t[e] += __shfl_xor(t[e], 8); t[e] += __shfl_xor(t[e], 16); t[e] += __shfl_xor(t[e], 32); }
  if (l < 8) {
    *(LAS f32x4*)(scratch + c.w * 256 + l * 32) = f32x4{t[0], t[1], t[2], t[3]};
    *(LAS f32x4*)(scratch + c.w * 256 + l * 32 + 16) = f32x4{t[4], t[5], t[6], t[7]};
  }
}
DI void pre_finish(const Ctx& c, u32 scratch) {
  const int j = (c.xcd & 1) * 32 + c.idx, chunk = j % 12, part = j / 12, qd = c.xcd >> 1;
  if (c.w == 7 && j < 60) {
    float s = 0.f;
#pragma unroll
    for (int i = 0; i < 8; ++i) s += *(LAS float*)(scratch + i * 256 + c.l * 4);
    const u32 col = (chunk / 6) * 1536 + 384 * qd + 64 * (chunk % 6) + c.l;
    st64<SC1>(v2u{__float_as_uint(s), c.tag}, c.rws, WS_CS + (part * 3072 + col) * 8);
  }
}

constexpr int NXS = 3, NWS = 7, SLOTB = 16384, PRE = 5;
constexpr bool qIsX(int q) { return q % 3 == 0; }
constexpr int qIdx(int q) { return q % 3 == 0 ? q / 3 : 2 * (q / 3) + (q % 3) - 1; }
constexpr int qSlotOff(int q) { return qIsX(q) ? (qIdx(q) % NXS) * SLOTB : (NXS + qIdx(q) % NWS) * SLOTB; }
constexpr int qFree(int q) { return qIsX(q) ? (qIdx(q) < NXS ? -1 : 2 * (qIdx(q) - NXS) + 2) : (qIdx(q) < NWS ? -1 : qIdx(q) - NWS + 1); }
constexpr int needQ(int st) { return 3 * (st / 2) + 1 + (st & 1); }
constexpr int issuedBefore(int st) {
  int n = PRE;
  for (int s = 0; s < st; s++)
    for (int j = 0; j < 2; j++)
      if (n < 30 && qFree(n) <= s) n++;
  return n;
}
constexpr u32 GLU_SCRATCH = 9 * SLOTB;
static_assert(qSlotOff(10) == GLU_SCRATCH && issuedBefore(2) <= 10, "scratch slot is free until barrier 2");

template <bool WITH_PRE> DI void glu_phase(const Ctx& c) {
  const int l = c.l, w = c.w;
  const int panel = (c.xcd & 1) * 8 + (c.idx & 7), sub = c.idx >> 3, grp = (c.xcd >> 1) * 3 + sub;
  const int rot = ((c.idx & 7) + 3 * sub) % 10;
  const rsrc_t rw = mkrs(w >= 4 ? c.wu : c.wg, 640 * 3072);
  Pre pre;
  if constexpr (WITH_PRE) pre_issue(c, pre);
  u32 xvo[2];
  sfor<2>([&](auto I) { constexpr int i = I; xvo[i] = (u32)((panel * 128 + 8 * (2 * w + i) + (l >> 3)) * 1280 + (((l & 7) ^ ((4 * i + (l >> 4)) & 7)) * 16)); });
  const u32 wvo = (u32)((8 * (w & 3) + (l >> 4)) * 3072 + grp * 256 + (((l & 15) ^ ((l >> 4) << 2)) * 16));
  const u32 m0b = c.lb + w * 2048;
  auto dma = [&](auto Q, auto I) {
    constexpr int q = Q, i = I;
    int blk = q / 3 + rot;
    blk = blk >= 10 ? blk - 10 : blk;
    if constexpr (qIsX(q)) DMA(m0b + qSlotOff(q) + i * 1024, xvo[i], c.rx, (u32)(blk * 128));
    else DMA(m0b + qSlotOff(q) + i * 1024, wvo, rw, (u32)((blk * 64 + (qIdx(q) & 1) * 32 + i * 4) * 3072));
  };
  if constexpr (WITH_PRE) {
    sfor<3>([&](auto Q) { sfor<2>([&](auto I) { dma(Q, I); }); });
    pre_compute(c, pre, c.lb + GLU_SCRATCH);
    sfor<PRE - 3>([&](auto Q) { sfor<2>([&](auto I) { dma(IC(Q + 3), I); }); });
  } else {
    sfor<PRE>([&](auto Q) { sfor<2>([&](auto I) { dma(Q, I); }); });
  }
  const int sh = w & 1, ch = w >> 1;
  u32 ta[3], xa[4];
  {
    const int qq = (l & 15) >> 2;
    sfor<3>([&](auto Z) { constexpr int z = Z; ta[z] = c.lb + z * 65536 + (u32)(qq * 256 + ((ch ^ qq) * 64) + ((l >> 4) & 1) * 32 + (l & 3) * 8 + (l >> 5) * 2048); OPAQUE(ta[z]); });
  }
  sfor<4>([&](auto M) { constexpr int m = M; xa[m] = c.lb + (u32)((sh * 64 + (l & 31)) * 128 + (((2 * m + (l >> 5)) ^ (((l & 31) >> 1) & 7)) * 16)); OPAQUE(xa[m]); });
  f32x16 acc[2][2];
  sfor<4>([&](auto T) { sfor<16>([&](auto E) { acc[T / 2][T % 2][(int)E] = 0.f; }); });
  bf16x8 wf[2][2], xf[2][2];
  auto rdw = [&](auto KS, auto T) {
    constexpr int ks = KS, t = T, st = ks >> 1;
    constexpr int off = (NXS + st % NWS) * SLOTB + t * 8192 + (ks & 1) * 4096;
    wf[ks & 1][t] = cat(trr(ta[off >> 16] + (off & 65535)), trr(ta[off >> 16] + (off & 65535) + 1024));
  };
  auto rdx = [&](auto KS, auto I) {
    constexpr int ks = KS, i = I, off = ((ks >> 2) % NXS) * SLOTB + i * 4096;
    xf[ks & 1][i] = *(LAS bf16x8*)(xa[ks & 3] + off);
  };
  WAITBAR<2 * (PRE - 1 - needQ(1))>();
  sfor<2>([&](auto I) { rdx(IC(0), I); });
  sfor<2>([&](auto T) { rdw(IC(0), T); });
  if constexpr (WITH_PRE) pre_finish(c, c.lb + GLU_SCRATCH);
  GAP();
  sfor<40>([&](auto KS) {
    constexpr int ks = KS, st = ks >> 1;
    if constexpr ((ks & 1) == 0 && ks > 0) {
      constexpr int n = st + 1 < 20 ? 2 * (issuedBefore(st) - 1 - needQ(st + 1)) : 0;
      static_assert(n >= 0, "");
      WAITBAR_V<n>();
    }
    constexpr int q0 = issuedBefore(st), q1 = issuedBefore(st + 1);
    sfor<4>([&](auto G) {
      constexpr int g = G, t = g / 2, i = g & 1;
      acc[i][t] = mfma32(wf[ks & 1][t], xf[ks & 1][i], acc[i][t]);
      if constexpr (ks + 1 < 40) {
        if constexpr (g < 2) rdx(IC(ks + 1), IC(g));
        else rdw(IC(ks + 1), IC(g - 2));
      }
      if constexpr (q0 + (ks & 1) < q1 && (g & 1) == 1) dma(IC(q0 + (ks & 1)), IC(g / 2));
      GAP();
    });
  });
  const int hh = l >> 5;
  v2u sg[2], cg[2][3];
  sfor<2>([&](auto I) { constexpr int i = I; sg[i] = ld64<SC1>(c.rws, WS_STAT + (panel * 128 + sh * 64 + i * 32 + (l & 31)) * 8); });
  sfor<6>([&](auto J) {
    constexpr int t = J / 3, j = J % 3;
    const int part = hh ? (j < 2 ? 3 + j : 4) : j;
    cg[t][j] = ld64<SC1>(c.rws, WS_CS + (part * 3072 + t * 1536 + grp * 128 + ch * 32 + (l & 31)) * 8);
  });
  bf16x8 cf[2], mf[2];
  float r2[2];
  sfor<2>([&](auto T) {
    constexpr int t = T;
    float s = __uint_as_float(cg[t][0].x) + __uint_as_float(cg[t][1].x) + (hh ? 0.f : __uint_as_float(cg[t][2].x));
    s += __shfl_xor(s, 32);
    const float h0 = rbf(s), h1 = rbf(s - h0), h2 = s - h0 - h1;
    cf[t] = asbf(v4u{hh ? 0u : cvtpk(h0, h1), hh ? 0u : cvtpk(h2, 0.f), 0u, 0u});
  });
  sfor<2>([&](auto I) {
    constexpr int i = I;
    const u32 nm = (sg[i].x & 0xffffu) ^ 0x8000u;
    mf[i] = asbf(v4u{nm | (nm << 16), nm, 0u, 0u});
    const float r = 1.0f / bfhi(sg[i].x);
    r2[i] = r * r;
  });
  sfor<4>([&](auto G) { constexpr int t = G / 2, i = G & 1; acc[i][t] = mfma32(cf[t], mf[i], acc[i][t]); });
  sfor<4>([&](auto G) {
    constexpr int i = G / 2, rg = G & 1;
    v4u o;
    sfor<4>([&](auto P) {
      constexpr int e = 8 * rg + 2 * P;
      o[(int)P] = cvtpk(acc[i][0][e] * acc[i][1][e] * r2[i], acc[i][0][e + 1] * acc[i][1][e + 1] * r2[i]);
    });
    const int rt = panel * 8 + sh * 4 + i * 2 + ((l >> 4) & 1), ks = grp * 4 + ch, q = 2 * rg + hh;
    st128<SC1>(o, c.rws, WS_H + (rt * 48 + ks) * 1024 + q * 256 + (l & 15) * 16);
  });
}

constexpr u32 SPC_X = 102400, SPC_SCR = SPC_X + 3 * SLOTB;
DI int spc_row(int panel, int R) {
  const int st = R >> 5, rho = R & 31, j = (rho & 3) + 4 * ((rho >> 3) & 1), hh = (rho >> 2) & 1, rg = rho >> 4;
  return 256 * (panel >> 1) + 32 * j + 16 * (panel & 1) + 4 * st + 2 * rg + hh;
}
DI void retile_w(const Ctx& c, int si) {
  for (int it = c.w; it < 12; it += 8) {
    const int k = 64 * (it >> 1) + c.l, cc = it & 1;
    const rsrc_t rs = mkrs(cc ? c.wi : c.wr, 2048 * 321 * 2);
    const u32 vo = k < 321 ? (u32)((si * 321 + k) * 2) : OOB;
    u32 v[32];
#pragma unroll
    for (int f2 = 0; f2 < 32; ++f2) v[f2] = __builtin_amdgcn_raw_buffer_load_b16(rs, vo, f2 * 64 * 321 * 2, 0);
    const u32 so = k < 321 ? WS_WT + k * 8192 + cc * 4096 + si * 64 : OOB;
#pragma unroll
    for (int q = 0; q < 4; ++q)
      st128<SC1>(v4u{v[4 * q] | (v[4 * q + 1] << 16), v[4 * q + 2] | (v[4 * q + 3] << 16), v[16 + 4 * q] | (v[17 + 4 * q] << 16), v[18 + 4 * q] | (v[19 + 4 * q] << 16)},
                 c.rws, so + q * 16);
  }
}
DI void retile_wd(const Ctx& c, int si, u32 buf) {
  const int item = si * 8 + c.w, l = c.l;
  if (item >= 480) return;
  const int ks = item / 10, cb = item % 10;
  const rsrc_t rs = mkrs(c.wd, 1536 * 1280);
  v4u v[4];
#pragma unroll
  for (int i = 0; i < 4; ++i) v[i] = ld128(rs, (32 * ks + 8 * i + (l >> 3)) * 1280 + cb * 128 + (l & 7) * 16);
  const int g = l >> 4;
  const u32 ra = buf + (4 * (g & 1) + ((l & 15) >> 2)) * 128 + (g >> 1) * 32 + (l & 3) * 8;
  bf16x4 t[4][2];
#pragma unroll
  for (int i = 0; i < 4; ++i) {
    *(LAS v4u*)(buf + l * 16) = v[i];
    CFENCE();
    t[i][0] = trr(ra);
    t[i][1] = trr(ra + 64);
    CFENCE();
  }
#pragma unroll
  for (int hp = 0; hp < 2; ++hp)
#pragma unroll
    for (int j = 0; j < 2; ++j) {
      const int c16 = cb * 4 + (g >> 1) + 2 * j, q = 2 * hp + (g & 1);
      st128<SC1>(__builtin_bit_cast(v4u, cat(t[2 * hp][j], t[2 * hp + 1][j])), c.rws,
                 WS_WD + (((c16 / 5) * 48 + ks) * 5 + c16 % 5) * 1024 + q * 256 + (l & 15) * 16);
    }
}
template <int T0, int NT> DI void spc_loop(const Ctx& c, int panel, int qd, const u32 (&xvo)[2]) {
  const int l = c.l, w = c.w, st = w & 3, hh = l >> 5;
  f32x16 acc[NT];
  sfor<NT>([&](auto T) { sfor<16>([&](auto E) { acc[T][(int)E] = 0.f; }); });
  u32 xa[4];
  sfor<4>([&](auto M) { constexpr int m = M; xa[m] = c.lb + SPC_X + (u32)((st * 32 + (l & 31)) * 128 + (((2 * m + hh) ^ (((l & 31) >> 1) & 7)) * 16)); });
  const u32 fa = c.lb + T0 * 20480 + l * 16;
  const u32 m0b = c.lb + SPC_X + w * 2048;
  sfor<10>([&](auto B) {
    constexpr int b = B;
    WAITBAR<(b < 9 ? 2 : 0)>();
    if constexpr (b + 2 < 10) sfor<2>([&](auto I) { DMA(m0b + ((b + 2) % 3) * SLOTB + I * 1024, xvo[I], c.rx, (u32)(((b + 2 + 5) % 10) * 128)); });
    if constexpr (b == 5) {
      const u32 sg = ((l >> 1) & 1) << 31;
      sfor<NT>([&](auto T) { sfor<16>([&](auto E) { acc[T][(int)E] = __uint_as_float(__float_as_uint(acc[T][(int)E]) ^ sg); }); });
    }
    sfor<4>([&](auto M) {
      constexpr int m = M;
      const bf16x8 xf = *(LAS bf16x8*)(xa[m] + (b % 3) * SLOTB);
      sfor<NT>([&](auto T) { acc[T] = mfma32(xf, *(LAS bf16x8*)(fa + T * 20480 + ((b % 5) * 4 + m) * 1024), acc[T]); });
    });
  });
  const v2u sg = ld64<SC1>(c.rws, WS_STAT + spc_row(panel, st * 32 + (l & 31)) * 8);
  const u32 scr = c.lb + SPC_SCR + 8192 + w * 256;
  if (l < 32) { *(LAS float*)(scr + l * 4) = 1.0f / bfhi(sg.x); *(LAS float*)(scr + 128 + l * 4) = bflo(sg.x); }
  CFENCE();
  f32x4 r[4];
  sfor<4>([&](auto G) { r[G] = *(LAS f32x4*)(scr + (8 * G + 4 * hh) * 4); });
  if (T0 == 0 && qd == 0) {
    sfor<4>([&](auto G) {
      const f32x4 m = *(LAS f32x4*)(scr + 128 + (8 * G + 4 * hh) * 4);
      sfor<4>([&](auto E) { acc[0][4 * G + E] -= (l & 31) == 0 ? 640.0f * m[(int)E] : 0.f; });
    });
  }
  sfor<NT * 2>([&](auto G) {
    constexpr int t = G / 2, rg = G & 1;
    v4u o;
    sfor<4>([&](auto P) {
      constexpr int e = 8 * rg + 2 * P;
      o[(int)P] = cvtpk(acc[t][e] * r[e >> 2][e & 3], acc[t][e + 1] * r[e >> 2][(e & 3) + 1]);
    });
    const int n = 160 * qd + 32 * (T0 + t) + (l & 31);
    st128<SC1>(o, c.rws, WS_Z + n * 4096 + (((panel & 1) * 8 + (panel >> 1)) * 16 + 4 * st + 2 * rg + hh) * 16);
  });
}
template <bool WITH_PRE> DI void spc_phase(const Ctx& c) {
  const int l = c.l, w = c.w;
  const int panel = (c.xcd & 1) * 8 + (c.idx & 7), qd = c.xcd >> 1;
  Pre pre;
  if constexpr (WITH_PRE) pre_issue(c, pre);
  u32 xvo[2];
  sfor<2>([&](auto I) { constexpr int i = I; xvo[i] = (u32)(spc_row(panel, 8 * (2 * w + i) + (l >> 3)) * 1280 + (((l & 7) ^ ((4 * i + (l >> 4)) & 7)) * 16)); });
  sfor<4>([&](auto J) { DMA(c.lb + SPC_X + w * 2048 + (J / 2) * SLOTB + (J & 1) * 1024, xvo[J & 1], c.rx, (u32)((5 + J / 2) * 128)); });
  for (int i = w; i < 100; i += 8) {
    const int t = i / 20, ks = i % 20, n = 160 * qd + 32 * t + (l & 31);
    const int k = n == 1 ? 320 : n >> 1;
    const float kf = (float)k * (1.0f / 640.0f), a0 = (float)((k * (16 * ks + 8 * (l >> 5))) % 640) * (1.0f / 640.0f) + ((n & 1) && n != 1 ? 0.25f : 0.f);
    v4u o;
#pragma unroll
    for (int p = 0; p < 4; ++p) o[p] = cvtpk(cosr(a0 + (2 * p) * kf), cosr(a0 + (2 * p + 1) * kf));
    *(LAS v4u*)(c.lb + i * 1024 + l * 16) = o;
  }
  if constexpr (WITH_PRE) {
    pre_compute(c, pre, c.lb + SPC_SCR + 10240);
    WAITBAR<63>();
    pre_finish(c, c.lb + SPC_SCR + 10240);
  }
  if (w < 4) spc_loop<0, 3>(c, panel, qd, xvo);
  else spc_loop<3, 2>(c, panel, qd, xvo);
}

struct FftC { bf16x8 C64[2], S64[2], N64[2], C32[2], S32[2], N32[2]; float t1c[8], t1s[8], t2c[8], t2s[8]; };
DI void fft_consts(FftC& f, int a, int l) {
  const int r = l & 15, q = l >> 4;
  sfor<2>([&](auto K) {
    constexpr int k = K;
    v4u c6, s6, c3, s3;
    sfor<4>([&](auto P) {
      constexpr int p = P;
      const float a0 = (float)(((16 * a + r) * (32 * k + 8 * q + 2 * p)) & 63) * (1.0f / 64), a1 = (float)(((16 * a + r) * (32 * k + 8 * q + 2 * p + 1)) & 63) * (1.0f / 64);
      c6[p] = cvtpk(cosr(a0), cosr(a1));
      s6[p] = cvtpk(sinr(a0), sinr(a1));
      const int s0 = 16 * (p >> 1) + 4 * q + 2 * (p & 1);
      const float b0 = (float)(((16 * k + r) * s0) & 31) * (1.0f / 32), b1 = (float)(((16 * k + r) * (s0 + 1)) & 31) * (1.0f / 32);
      c3[p] = cvtpk(cosr(b0), cosr(b1));
      s3[p] = cvtpk(sinr(b0), sinr(b1));
    });
    f.C64[k] = asbf(c6); f.S64[k] = asbf(s6); f.N64[k] = asbf(s6 ^ 0x80008000u);
    f.C32[k] = asbf(c3); f.S32[k] = asbf(s3); f.N32[k] = asbf(s3 ^ 0x80008000u);
  });
  sfor<8>([&](auto E) {
    constexpr int e = E;
    const float a1 = (float)((16 * (e >> 2) + 4 * q + (e & 3)) * (16 * a + r)) * (1.0f / 2048);
    const float a2 = (float)((16 * a + 4 * q + (e & 3)) * (16 * (e >> 2) + r)) * (1.0f / 2048);
    f.t1c[e] = cosr(a1); f.t1s[e] = sinr(a1); f.t2c[e] = cosr(a2); f.t2s[e] = sinr(a2);
  });
}
DI bf16x8 pack8(const float (&v)[8]) { return asbf(v4u{cvtpk(v[0], v[1]), cvtpk(v[2], v[3]), cvtpk(v[4], v[5]), cvtpk(v[6], v[7])}); }
template <class SYNC> DI void fft_column(const Ctx& c, const FftC& f, int a, int k, u32 ex, SYNC&& group_sync) {
  const int l = c.l, r = l & 15, q = l >> 4;
  const bool real = k == 0 || k == 320;
  const u32 pr = k == 320 ? 4096u : (u32)k * 8192u, io = real ? OOB : 4096u;
  bf16x8 zr[4], zi[4];
  sfor<4>([&](auto I) { zr[I] = asbf(ld128<SC1>(c.rws, l * 16 + I * 1024, WS_Z + pr)); zi[I] = asbf(ld128<SC1>(c.rws, io + l * 16 + I * 1024, WS_Z + pr)); });
  const v4u wr = ld128<SC1>(c.rws, (16 * a + r) * 64 + q * 16, WS_WT + k * 8192), wi = ld128<SC1>(c.rws, (16 * a + r) * 64 + q * 16 + 4096, WS_WT + k * 8192);
  const f32x4 z4 = {0.f, 0.f, 0.f, 0.f};
  float xr[8], xi[8];
  sfor<2>([&](auto T) {
    constexpr int t = T;
    f32x4 re = z4, im = z4;
    sfor<2>([&](auto K) {
      re = mfma16(zr[2 * t + K], f.C64[K], re); im = mfma16(zi[2 * t + K], f.C64[K], im);
      re = mfma16(zi[2 * t + K], f.S64[K], re); im = mfma16(zr[2 * t + K], f.N64[K], im);
    });
    sfor<4>([&](auto E) { constexpr int e = 4 * t + E; xr[e] = re[(int)E] * f.t1c[e] + im[(int)E] * f.t1s[e]; xi[e] = im[(int)E] * f.t1c[e] - re[(int)E] * f.t1s[e]; });
  });
  bf16x8 br = pack8(xr), bi = pack8(xi);
  const float sc = (real ? 1.0f : 2.0f) / (2048.0f * 640.0f);
  sfor<2>([&](auto U) {
    constexpr int u = U;
    f32x4 re = mfma16(f.C32[u], br, z4), im = mfma16(f.C32[u], bi, z4);
    re = mfma16(f.S32[u], bi, re); im = mfma16(f.N32[u], br, im);
    sfor<4>([&](auto E) {
      constexpr int e = 4 * u + E;
      const float p = ((e & 1) ? bfhi(wr[e >> 1]) : bflo(wr[e >> 1])) * sc, s = ((e & 1) ? bfhi(wi[e >> 1]) : bflo(wi[e >> 1])) * sc;
      xr[e] = re[(int)E] * p - im[(int)E] * s; xi[e] = re[(int)E] * s + im[(int)E] * p;
    });
  });
  br = pack8(xr); bi = pack8(xi);
  sfor<2>([&](auto V) {
    constexpr int v = V;
    f32x4 re = mfma16(br, f.C32[v], z4), im = mfma16(bi, f.C32[v], z4);
    re = mfma16(bi, f.N32[v], re); im = mfma16(br, f.S32[v], im);
    float ur[4], ui[4];
    sfor<4>([&](auto E) { constexpr int e = 4 * v + E; ur[E] = re[(int)E] * f.t2c[e] - im[(int)E] * f.t2s[e]; ui[E] = im[(int)E] * f.t2c[e] + re[(int)E] * f.t2s[e]; });
    const u32 wa = ex + ((((r >> 2) & 1) * 2 + (a >> 1)) * 64 + (2 * (a & 1) + (q >> 1)) * 16 + 4 * (2 * v + (r >> 3)) + (r & 3)) * 16 + (q & 1) * 8;
    *(LAS v2u*)wa = v2u{cvtpk(ur[0], ur[1]), cvtpk(ur[2], ur[3])};
    *(LAS v2u*)(wa + 4096) = v2u{cvtpk(ui[0], ui[1]), cvtpk(ui[2], ui[3])};
  });
  group_sync();
  sfor<2>([&](auto V) {
    constexpr int v = V;
    f32x4 re = z4, im = z4;
    sfor<2>([&](auto K) {
      const bf16x8 er = *(LAS bf16x8*)(ex + (v * 2 + K) * 1024 + l * 16), ei = *(LAS bf16x8*)(ex + 4096 + (v * 2 + K) * 1024 + l * 16);
      re = mfma16(er, f.C64[K], re); im = mfma16(ei, f.C64[K], im);
      re = mfma16(ei, f.N64[K], re); im = mfma16(er, f.S64[K], im);
    });
    sfor<4>([&](auto E) { xr[4 * v + E] = re[(int)E]; xi[4 * v + E] = im[(int)E]; });
  });
  st128<SC1>(__builtin_bit_cast(v4u, pack8(xr)), c.rws, (16 * a + r) * 64 + q * 16, WS_Y + pr);
  st128<SC1>(__builtin_bit_cast(v4u, pack8(xi)), c.rws, io + (16 * a + r) * 64 + q * 16, WS_Y + pr);
}

constexpr u32 OUT_A = 0, OUT_B = 81920, OUT_XA = OUT_B + 61440;
template <class SYNC> DI void out_a(const Ctx& c, SYNC&& group_sync) {
  const int l = c.l, a = c.w, q = l >> 4, r = l & 15;
  const int rt = 4 * c.xcd + (c.idx & 3), ct = c.idx >> 2;
  v2u xres[5];
  sfor<5>([&](auto T) { xres[T] = ld64(c.rx, (rt * 64 + a * 16 + r) * 1280 + (ct * 80 + 16 * T + 4 * q) * 2); });
  bf16x8 g[5][5];
  sfor<5>([&](auto T) {
    int d = ct * 80 + 16 * T + r;
    d = d > 320 ? d - 640 : d;
    const float df = (float)d * (1.0f / 640.0f);
    sfor<5>([&](auto KS) {
      v4u o;
      sfor<4>([&](auto P) {
        constexpr int p = P;
        const int k = 16 * (5 * a + KS) + 8 * (p >> 1) + 2 * q + (p & 1);
        const float an = (float)k * df;
        float sn = -sinr(an);
        if constexpr (KS == 0 && p == 0) sn = k == 0 ? ((d & 1) ? -1.0f : 1.0f) : sn;
        o[p] = cvtpk(cosr(an), sn);
      });
      g[T][KS] = asbf(o);
    });
  });
  const u32 yb = c.lb + OUT_A + a * 20480;
  v4u y[20];
  sfor<20>([&](auto I) { y[I] = ld128<SC1>(c.rws, (l >> 3) * 4096 + (l & 7) * 16, WS_Y + (160 * a + 8 * I) * 4096 + rt * 128); });
  sfor<20>([&](auto I) { *(LAS v4u*)(yb + I * 1024 + (l >> 3) * 128 + (((l & 7) ^ (((l >> 4) & 3) << 1)) * 16)) = y[I]; });
  CFENCE();
  f32x4 acc[5][4];
  const int rr = 4 * q + (r >> 2);
  sfor<5>([&](auto KS) {
    sfor<4>([&](auto ST) {
      const u32 ad = yb + KS * 4096 + rr * 128 + ((ST ^ ((rr >> 1) & 3)) * 32) + (l & 3) * 8;
      const bf16x8 yf = cat(trr(ad), trr(ad + 2048));
      sfor<5>([&](auto T) { acc[T][ST] = mfma16(g[T][KS], yf, KS == 0 ? f32x4{0.f, 0.f, 0.f, 0.f} : acc[T][ST]); });
    });
  });
  CFENCE();
  sfor<20>([&](auto I) { *(LAS f32x4*)(yb + I * 1024 + l * 16) = acc[I % 5][I / 5]; });
  group_sync();
  sfor<5>([&](auto T) {
    f32x4 s = {0.f, 0.f, 0.f, 0.f};
    sfor<4>([&](auto J) { s += *(LAS f32x4*)(c.lb + OUT_A + J * 20480 + (a * 5 + T) * 1024 + l * 16); });
    const f32x4 o = {rbf(bflo(xres[T].x) + rbf(s[0])), rbf(bfhi(xres[T].x) + rbf(s[1])), rbf(bflo(xres[T].y) + rbf(s[2])), rbf(bfhi(xres[T].y) + rbf(s[3]))};
    *(LAS f32x4*)(yb + (a * 5 + T) * 1024 + l * 16) = o;
  });
  WAITBAR<63>();
}
DI void out_b(const Ctx& c) {
  const int l = c.l, b = c.w - 4, q = l >> 4, r = l & 15;
  const int rt = 4 * c.xcd + (c.idx & 3), ct = c.idx >> 2;
  constexpr int WW = 5, HW = 3;
  const u32 wo = WS_WD + (ct * 48 + 12 * b) * 5120, ho = WS_H + (rt * 4 * 48 + 12 * b) * 1024;
  v4u wd[WW][5], hq[HW][4];
  sfor<WW>([&](auto I) { sfor<5>([&](auto T) { wd[I][T] = ld128<SC1>(c.rws, l * 16 + T * 1024, wo + I * 5120); }); });
  GAP();
  sfor<HW>([&](auto I) { sfor<4>([&](auto ST) { hq[I][ST] = ld128<SC1>(c.rws, l * 16, ho + ST * 49152 + I * 1024); }); });
  GAP();
  f32x4 acc[5][4];
  sfor<12>([&](auto I) {
    constexpr int i = I;
    sfor<4>([&](auto ST) {
      sfor<5>([&](auto T) { acc[T][ST] = mfma16(asbf(wd[i % WW][T]), asbf(hq[i % HW][ST]), i == 0 ? f32x4{0.f, 0.f, 0.f, 0.f} : acc[T][ST]); });
      if constexpr (i + HW < 12) hq[i % HW][ST] = ld128<SC1>(c.rws, l * 16, ho + ST * 49152 + (i + HW) * 1024);
      GAP();
    });
    if constexpr (i + WW < 12) sfor<5>([&](auto T) { wd[i % WW][T] = ld128<SC1>(c.rws, l * 16 + T * 1024, wo + (i + WW) * 5120); });
    GAP();
  });
  f32x4 own[5];
  sfor<4>([&](auto ST) {
    const int st = ST;
    if (st == b) { sfor<5>([&](auto T) { own[T] = acc[T][ST]; }); }
    else {
      const u32 ad = c.lb + OUT_B + (st * 3 + (b > st ? b - 1 : b)) * 5120 + l * 16;
      sfor<5>([&](auto T) { *(LAS f32x4*)(ad + T * 1024) = acc[T][ST]; });
    }
  });
  WAITBAR<63>();
  const rsrc_t ro = mkrs(c.out, 2048 * 1280);
  sfor<5>([&](auto T) {
    f32x4 s = own[T];
    sfor<3>([&](auto J) { s += *(LAS f32x4*)(c.lb + OUT_B + (b * 3 + J) * 5120 + T * 1024 + l * 16); });
    const f32x4 xa = *(LAS f32x4*)(c.lb + OUT_A + b * 20480 + (b * 5 + T) * 1024 + l * 16);
    st64(v2u{cvtpk(xa[0] + rbf(s[0]), xa[1] + rbf(s[1])), cvtpk(xa[2] + rbf(s[2]), xa[3] + rbf(s[3]))}, ro,
         (rt * 64 + b * 16 + r) * 1280 + (ct * 80 + 16 * T + 4 * q) * 2);
  });
}

DI void group_barrier(u32 cnt, u32 target, int l) {
  asm volatile("s_waitcnt lgkmcnt(0)" ::: "memory");
  if (l == 0) __hip_atomic_fetch_add((LAS u32*)cnt, 1u, __ATOMIC_RELAXED, __HIP_MEMORY_SCOPE_WORKGROUP);
  while (__hip_atomic_load((LAS u32*)cnt, __ATOMIC_RELAXED, __HIP_MEMORY_SCOPE_WORKGROUP) < target) __builtin_amdgcn_s_sleep(0);
  CFENCE();
}

constexpr u32 LDS_BYTES = 163840, CNT_OFF = 163840 - 64, EX_OFF = 143360 - 32768;

template <int PH>
__global__ __launch_bounds__(512, 2) void k_phase(const u16* x, const u16* wr, const u16* wi, const u16* wg, const u16* wu, const u16* wd, u16* out, char* ws) {
  extern __shared__ __attribute__((aligned(16))) char shm[];
  Ctx c;
  c.rx = mkrs(x, 2048 * 1280);
  c.rws = mkrs(ws, WS_END);
  c.wr = wr; c.wi = wi; c.wg = wg; c.wu = wu; c.wd = wd; c.out = out;
  c.lb = (u32)(size_t)shm;
  c.tag = 1;
  c.l = threadIdx.x & 63;
  c.w = __builtin_amdgcn_readfirstlane(threadIdx.x >> 6);
  c.xcd = blockIdx.x & 7;
  c.idx = blockIdx.x >> 3;
  const bool spc = (c.idx >> 3) == 3;
  const int si = (c.xcd >> 1) * 16 + (c.xcd & 1) * 8 + (c.idx & 7), gi = c.xcd * 24 + c.idx;
  if constexpr (PH == 0) {
    Pre pre;
    pre_issue(c, pre);
    pre_compute(c, pre, c.lb);
    __syncthreads();
    pre_finish(c, c.lb);
    if (spc) { retile_w(c, si); retile_wd(c, si, c.lb + 4096 + c.w * 1024); }
  }
  if constexpr (PH == 1) {
    if (spc) spc_phase<false>(c);
    else glu_phase<false>(c);
  }
  if constexpr (PH == 2) {
    if (threadIdx.x < 4) *(LAS u32*)(c.lb + CNT_OFF + threadIdx.x * 4) = 0;
    __syncthreads();
    const int grp = c.w >> 2, a = c.w & 3;
    if (spc || grp == 0) {
      FftC f;
      fft_consts(f, a, c.l);
      const int k0 = spc ? 192 + 2 * si + grp : gi, n = k0 == 319 ? 2 : 1;
      for (int i = 0; i < n; ++i)
        fft_column(c, f, a, k0 + i, c.lb + EX_OFF + grp * 16384 + i * 8192, [&]() { group_barrier(c.lb + CNT_OFF + grp * 4, 4 * (i + 1), c.l); });
    }
  }
  if constexpr (PH == 3) {
    if (threadIdx.x < 4) *(LAS u32*)(c.lb + CNT_OFF + threadIdx.x * 4) = 0;
    __syncthreads();
    if (c.w < 4) out_a(c, [&]() { group_barrier(c.lb + CNT_OFF + 8, 4, c.l); });
    else out_b(c);
  }
}

extern "C" void kernel_launch(void* const* d_in, const int* in_sizes, int n_in, void* d_out, int out_size, void* d_ws, size_t ws_size, hipStream_t stream) {
  const u16* x = (const u16*)d_in[0];
  const u16* wr = (const u16*)d_in[1];
  const u16* wi = (const u16*)d_in[2];
  const u16* wg = (const u16*)d_in[4];
  const u16* wu = (const u16*)d_in[5];
  const u16* wd = (const u16*)d_in[6];
  (void)hipFuncSetAttribute((const void*)k_phase<0>, hipFuncAttributeMaxDynamicSharedMemorySize, LDS_BYTES);
  (void)hipFuncSetAttribute((const void*)k_phase<1>, hipFuncAttributeMaxDynamicSharedMemorySize, LDS_BYTES);
  (void)hipFuncSetAttribute((const void*)k_phase<2>, hipFuncAttributeMaxDynamicSharedMemorySize, LDS_BYTES);
  (void)hipFuncSetAttribute((const void*)k_phase<3>, hipFuncAttributeMaxDynamicSharedMemorySize, LDS_BYTES);
  k_phase<0><<<256, 512, LDS_BYTES, stream>>>(x, wr, wi, wg, wu, wd, (u16*)d_out, (char*)d_ws);
  k_phase<1><<<256, 512, LDS_BYTES, stream>>>(x, wr, wi, wg, wu, wd, (u16*)d_out, (char*)d_ws);
  k_phase<2><<<256, 512, LDS_BYTES, stream>>>(x, wr, wi, wg, wu, wd, (u16*)d_out, (char*)d_ws);
  k_phase<3><<<256, 512, LDS_BYTES, stream>>>(x, wr, wi, wg, wu, wd, (u16*)d_out, (char*)d_ws);
}
```

```cpp
#include <hip/hip_runtime.h>
#include <utility>

typedef unsigned u32;
typedef unsigned short u16;
typedef unsigned long long u64;
typedef unsigned v4u __attribute__((ext_vector_type(4)));
typedef unsigned v2u __attribute__((ext_vector_type(2)));
typedef float f32x2 __attribute__((ext_vector_type(2)));
typedef float f32x4 __attribute__((ext_vector_type(4)));
typedef __bf16 bf16x2 __attribute__((ext_vector_type(2)));
typedef __bf16 bf16x4 __attribute__((ext_vector_type(4)));
typedef __bf16 bf16x8 __attribute__((ext_vector_type(8)));
typedef __amdgpu_buffer_rsrc_t rsrc_t;
#define LAS __attribute__((address_space(3)))
#define GAS __attribute__((address_space(1)))
#define DI __device__ __forceinline__
#define GAP() __builtin_amdgcn_sched_barrier(0)
#define IC(x) std::integral_constant<int, (x)>{}
#pragma clang diagnostic ignored "-Wint-to-pointer-cast"

template <class F, int... Is> DI void sfor_(F&& f, std::integer_sequence<int, Is...>) { (f(std::integral_constant<int, Is>{}), ...); }
template <int N, class F> DI void sfor(F&& f) { sfor_(f, std::make_integer_sequence<int, N>{}); }

constexpr u32 WS_STAT = 0x4000;
constexpr u32 WS_CS = 0x8000;
constexpr u32 WS_FLAG = 0x20000, F_Z = 0, F_H = 192, F_W = 384, F_Y = 448, F_A = 512;
constexpr u32 WS_Z = 0x40000;
constexpr u32 WS_Y = WS_Z + 0x300000;
constexpr u32 WS_WD = WS_Y + 0x300000;
constexpr u32 WS_H = WS_WD + 0x1E0000;
constexpr u32 WS_A = WS_H + 0x600000;
constexpr u32 WS_END = WS_A + 0x280000;
constexpr u32 OOB = 0x40000000u;
constexpr int SC1 = 16;
constexpr u32 SPIN_CAP = 1u << 16;
constexpr u32 LDS_BYTES = 163840, SLOTB = 16384;
constexpr u32 L_CTL = LDS_BYTES - 256;
enum { C_ARRZ = 0, C_ARRW = 1, C_ARRY = 3, C_ARRA = 4, C_GOZ = 5, C_GOY = 6, C_GOH = 7, C_GOA = 8, C_OUTA = 9, C_N = 16 };

DI u32 cvtpk(float lo, float hi) { f32x2 v = {lo, hi}; return __builtin_bit_cast(u32, __builtin_convertvector(v, bf16x2)); }
DI float bflo(u32 v) { return __uint_as_float(v << 16); }
DI float bfhi(u32 v) { return __uint_as_float(v & 0xffff0000u); }
DI float rbf(float f) { return bflo(cvtpk(f, f)); }
DI bf16x8 asbf(v4u v) { return __builtin_bit_cast(bf16x8, v); }
DI v4u asu(bf16x8 v) { return __builtin_bit_cast(v4u, v); }
DI rsrc_t mkrs(const void* p, u32 n) { return __builtin_amdgcn_make_buffer_rsrc((void*)p, 0, n, 0x00020000); }
DI void DMA(u32 m0v, u32 voff, rsrc_t rs, u32 soff) {
  asm volatile("s_mov_b32 m0, %0\n\ts_nop 0\n\tbuffer_load_dwordx4 %1, %2, %3 offen lds" ::"s"(m0v), "v"(voff), "s"(rs), "s"(soff) : "memory");
}
DI void BLD(v4u& d, u32 voff, rsrc_t rs, u32 soff) { asm volatile("buffer_load_dwordx4 %0, %1, %2, %3 offen" : "=v"(d) : "v"(voff), "s"(rs), "s"(soff) : "memory"); }
DI void BLD32(u32& d, u32 voff, rsrc_t rs) { asm volatile("buffer_load_dword %0, %1, %2, 0 offen sc1" : "=v"(d) : "v"(voff), "s"(rs) : "memory"); }
template <int N> DI void WAITBAR() { asm volatile("s_waitcnt vmcnt(%0) lgkmcnt(0)\n\ts_barrier" ::"i"(N) : "memory"); }
DI void DRAIN() { asm volatile("s_waitcnt vmcnt(0)" ::: "memory"); }
DI void OPAQUE(u32& v) { asm volatile("" : "+v"(v)); }
DI void PIN(bf16x8& v) { asm volatile("" : "+v"(v)); }
DI void PIN(v4u& v) { asm volatile("" : "+v"(v)); }
DI void PIN(f32x4& v) { asm volatile("" : "+v"(v)); }
DI void PIN(float& v) { asm volatile("" : "+v"(v)); }
DI void CFENCE() { asm volatile("" ::: "memory"); }
DI bf16x4 trr(u32 a) { return __builtin_amdgcn_ds_read_tr16_b64_v4bf16((LAS bf16x4*)a); }
DI bf16x8 cat(bf16x4 a, bf16x4 b) { return __builtin_shufflevector(a, b, 0, 1, 2, 3, 4, 5, 6, 7); }
DI f32x4 mfma16(bf16x8 a, bf16x8 b, f32x4 c) { return __builtin_amdgcn_mfma_f32_16x16x32_bf16(a, b, c, 0, 0, 0); }
template <int AUX = 0> DI v4u ld128(rsrc_t rs, u32 voff, u32 soff = 0) { return __builtin_amdgcn_raw_buffer_load_b128(rs, voff, soff, AUX); }
template <int AUX = 0> DI v2u ld64(rsrc_t rs, u32 voff, u32 soff = 0) { return __builtin_amdgcn_raw_buffer_load_b64(rs, voff, soff, AUX); }
template <int AUX = 0> DI u32 ld32(rsrc_t rs, u32 voff, u32 soff = 0) { return __builtin_amdgcn_raw_buffer_load_b32(rs, voff, soff, AUX); }
template <int AUX = 0> DI void st128(v4u v, rsrc_t rs, u32 voff, u32 soff = 0) { __builtin_amdgcn_raw_buffer_store_b128(v, rs, voff + soff, 0, AUX); }
template <int AUX = 0> DI void st64(v2u v, rsrc_t rs, u32 voff, u32 soff = 0) { __builtin_amdgcn_raw_buffer_store_b64(v, rs, voff, soff, AUX); }
template <int AUX = 0> DI void st32(u32 v, rsrc_t rs, u32 voff, u32 soff = 0) { __builtin_amdgcn_raw_buffer_store_b32(v, rs, voff, soff, AUX); }
DI float cosr(float rev) { return __builtin_amdgcn_cosf(rev); }
DI float sinr(float rev) { return __builtin_amdgcn_sinf(rev); }
DI float rev640(int p) {
  const float pf = (float)p;
  return __builtin_fmaf(__builtin_floorf(pf * (1.0f / 640.0f)), -640.0f, pf) * (1.0f / 640.0f);
}
template <int CTRL> DI float dpp(float v) { return __int_as_float(__builtin_amdgcn_update_dpp(0, __float_as_int(v), CTRL, 0xf, 0xf, true)); }
DI float fold32(float a, float b) {
  const auto r = __builtin_amdgcn_permlane32_swap(__float_as_uint(a), __float_as_uint(b), false, false);
  return __uint_as_float(r[0]) + __uint_as_float(r[1]);
}
DI float fold16(float a, float b) {
  const auto r = __builtin_amdgcn_permlane16_swap(__float_as_uint(a), __float_as_uint(b), false, false);
  return __uint_as_float(r[0]) + __uint_as_float(r[1]);
}
DI float wave_sum(float v) {
  v += dpp<0xB1>(v); v += dpp<0x4E>(v); v += dpp<0x141>(v); v += dpp<0x140>(v);
  v = fold16(v, v);
  return fold32(v, v);
}

#define STAMP(i)
struct Ctx {
  char* ws;
  rsrc_t rx, rws;
  const u16 *wr, *wi, *wg, *wu, *wd;
  u16* out;
  static constexpr u32 lb = 0;
  u32 tag;
  int l, w, xcd, idx;
};

DI u32 lds_ld(u32 a) { return __hip_atomic_load((LAS u32*)a, __ATOMIC_RELAXED, __HIP_MEMORY_SCOPE_WORKGROUP); }
DI void group_barrier(const Ctx& c, int word, u32 target) {
  asm volatile("s_waitcnt lgkmcnt(0)" ::: "memory");
  if (c.l == 0) __hip_atomic_fetch_add((LAS u32*)(c.lb + L_CTL + word * 4), 1u, __ATOMIC_RELAXED, __HIP_MEMORY_SCOPE_WORKGROUP);
#pragma nounroll
  for (u32 n = 0; n < (SPIN_CAP << 8) && lds_ld(c.lb + L_CTL + word * 4) < target; ++n) __builtin_amdgcn_s_sleep(1);
  CFENCE();
}
DI void raise(const Ctx& c, u32 flag) { if (c.l == 0) st32<SC1>(c.tag, c.rws, WS_FLAG + flag * 128); }
template <int N = 0> DI void arrive_publish(const Ctx& c, int word, u32 flag) {
  asm volatile("s_waitcnt vmcnt(%0)" ::"i"(N) : "memory");
  u32 old = 0;
  if (c.l == 0) old = __hip_atomic_fetch_add((LAS u32*)(L_CTL + word * 4), 1u, __ATOMIC_RELAXED, __HIP_MEMORY_SCOPE_WORKGROUP);
  old = __builtin_amdgcn_readfirstlane(old);
  if (old == 7u) raise(c, flag);
}
DI void poll_flags(const Ctx& c, u32 flag) {
#pragma nounroll
  for (u32 i = 0; i < SPIN_CAP && ld32<SC1>(c.rws, WS_FLAG + flag * 128) != c.tag; ++i) __builtin_amdgcn_s_sleep(2);
  CFENCE();
}
DI void flag_wait(const Ctx& c, bool leader, int word, u32 flag) {
  if (leader) {
    poll_flags(c, flag);
    if (c.l == 0) __hip_atomic_store((LAS u32*)(L_CTL + word * 4), 1u, __ATOMIC_RELAXED, __HIP_MEMORY_SCOPE_WORKGROUP);
  } else {
#pragma nounroll
    for (u32 i = 0; i < (SPIN_CAP << 6) && lds_ld(L_CTL + word * 4) == 0u; ++i) __builtin_amdgcn_s_sleep(1);
    CFENCE();
  }
}
DI void tag_wait(Ctx& c, u32& epoch) {
  asm volatile("s_waitcnt lgkmcnt(0)" : "+s"(epoch));
  epoch += 1u;
  c.tag = (epoch * 0x9E3779B1u) ^ 0x5bd1e995u;
}
DI int out_kw(const Ctx& c) { return (c.w + (c.idx >> 2) + 2 * (c.idx & 3)) & 7; }
template <int I> DI void wd_load(const Ctx& c, v4u (&wd)[30]) {
  wd[I] = ld128<SC1>(c.rws, c.l * 16 + (I % 4) * 1024, WS_WD + ((c.idx >> 2) * 48 + 6 * out_kw(c)) * 5120 + (I / 4) * 4096);
}

DI bf16x8 pack8(const float (&v)[8]) { return asbf(v4u{cvtpk(v[0], v[1]), cvtpk(v[2], v[3]), cvtpk(v[4], v[5]), cvtpk(v[6], v[7])}); }
DI bf16x8 pack44(f32x4 a, f32x4 b) { return asbf(v4u{cvtpk(a[0], a[1]), cvtpk(a[2], a[3]), cvtpk(b[0], b[1]), cvtpk(b[2], b[3])}); }
DI v2u pack4(f32x4 a) { return v2u{cvtpk(a[0], a[1]), cvtpk(a[2], a[3])}; }
DI bf16x8 neg8(bf16x8 v) { return asbf(asu(v) ^ 0x80008000u); }
DI float dot2(u32 a, u32 b, float c) { return __builtin_amdgcn_fdot2_f32_bf16(__builtin_bit_cast(bf16x2, a), __builtin_bit_cast(bf16x2, b), c, false); }
DI bf16x8 cos8(float a0, float da) {
  float cc[8];
  sfor<8>([&](auto E) { cc[E] = cosr(__builtin_fmaf(da, (float)E, a0)); });
  return pack8(cc);
}
DI void tile_tr(const Ctx& c, const v4u (&v)[4], v4u (&f)[4], u32 buf) {
  const int l = c.l, rr = 4 * (l >> 4) + ((l & 15) >> 2);
  const u32 wa = buf + (l >> 3) * 128 + (((l & 7) ^ (((l >> 4) & 3) << 1)) * 16), ra = buf + rr * 128 + (l & 3) * 8;
  bf16x4 t[2][4];
#pragma unroll
  for (int u = 0; u < 2; ++u) {
    *(LAS v4u*)wa = v[2 * u];
    *(LAS v4u*)(wa + 1024) = v[2 * u + 1];
    CFENCE();
#pragma unroll
    for (int j = 0; j < 4; ++j) t[u][j] = trr(ra + ((j ^ ((rr >> 1) & 3)) * 32));
    CFENCE();
  }
#pragma unroll
  for (int j = 0; j < 4; ++j) f[j] = asu(cat(t[0][j], t[1][j]));
}
struct Cos640 { u16 v[640]; };
constexpr Cos640 make_cos() {
  Cos640 t{};
  for (int i = 0; i < 640; ++i) {
    int n = i > 320 ? 640 - i : i;
    const bool neg = n > 160;
    if (neg) n = 320 - n;
    const double x = 6.283185307179586476925 * n / 640.0, x2 = x * x;
    double term = 1.0, sum = 1.0;
    for (int j = 1; j <= 12; ++j) { term *= -x2 / ((2 * j - 1) * (2 * j)); sum += term; }
    const float f = n == 160 ? 0.f : (float)(neg ? -sum : sum);
    const u32 u = __builtin_bit_cast(u32, f);
    t.v[i] = (u16)((u + 0x7fffu + ((u >> 16) & 1u)) >> 16);
  }
  return t;
}
constexpr Cos640 COS = make_cos();
constexpr int col_kc(int g, int m) { return g == 20 ? 320 : (g == 0 ? (m < 8 ? 40 * m : 40 * m - 300) : (g + 40 * m > 320 ? 640 - g - 40 * m : g + 40 * m)); }
constexpr u16 m_at(int g, int cc, int m, int cp, int d2) {
  const bool edge = g == 0 || g == 20, on = !edge || (g == 0 ? (m >> 3) == cp : (m == 0 && cp == 0));
  return on ? COS.v[(col_kc(g, m) * d2 + 160 * cc + ((cp && !edge) ? (g + 40 * m > 320 ? 160 : 480) : 0)) % 640] : (u16)0;
}
struct MGroup { u16 v[4][512]; };
constexpr MGroup make_m(int g) {
  MGroup t{};
  for (int h = 0; h < 2; ++h)
    for (int l = 0; l < 64; ++l)
      for (int e = 0; e < 8; ++e) {
        t.v[h][l * 8 + e] = m_at(g, h, l & 15, l >> 5, 8 * ((l >> 4) & 1) + e);
        t.v[2 + h][l * 8 + e] = m_at(g, e >> 2, 4 * (l >> 4) + (e & 3), h, l & 15);
      }
  return t;
}
template <int G> constexpr MGroup M_OF = make_m(G);
struct Small { u16 s[6][1024]; u16 f[8][512]; };
constexpr Small make_small() {
  Small t{};
  for (int w = 0; w < 6; ++w)
    for (int l = 0; l < 64; ++l)
      for (int e = 0; e < 12; ++e) {
        const int k = 16 * (e >> 2) + 4 * (l >> 4) + (e & 3), j = w < 3 ? 14 * w + (l & 15) : k, d1 = w < 3 ? k : 16 * (w - 3) + (l & 15);
        const int g = j == 1 ? 20 : (j == 40 ? 0 : j >> 1);
        t.s[w][(e >> 3) * 512 + l * 8 + (e & 7)] = (j <= 40 && d1 < 40) ? COS.v[(16 * g * d1 + (((j & 1) && j != 1) ? 160 : 0)) % 640] : (u16)0;
      }
  for (int w = 0; w < 8; ++w)
    for (int l = 0; l < 64; ++l)
      for (int e = 0; e < 8; ++e)
        t.f[w][l * 8 + e] = COS.v[(10 * (2 * (l & 15) + ((w >> 1) & 1)) * (2 * (4 * (l >> 4) + (e & 3)) + (w & 1) + 32 * (e >> 2)) + (w >= 4 ? 480 : 0)) % 640];
  return t;
}
constexpr Small SMALL = make_small();
struct Tables { MGroup m[21]; Small s; };
template <int... G> constexpr Tables make_tables(std::integer_sequence<int, G...>) { return Tables{{M_OF<G>...}, SMALL}; }
static __device__ __attribute__((aligned(128))) const Tables TABLES = make_tables(std::make_integer_sequence<int, 21>{});
constexpr u32 T_S = 21 * 4096, T_F = T_S + 12288, T_END = T_F + 8192;
static_assert(sizeof(Tables) == T_END, "");

DI int col_k(int g, int m) {
  const int kp = g + 40 * m;
  return g == 20 ? 320 : (g == 0 ? (m < 8 ? kp : kp - 300) : (kp > 320 ? 640 - kp : kp));
}
template <int NT> DI void stage40(u32 a, u32 tail, const v4u (&f)[NT][2], f32x4 (&acc)[NT]) {
  const bf16x8 x0 = cat(trr(a), trr(a + 512)), x1 = cat(trr(tail), trr(tail));
  sfor<NT>([&](auto T) { acc[T] = mfma16(x1, asbf(f[T][1]), mfma16(x0, asbf(f[T][0]), f32x4{0.f, 0.f, 0.f, 0.f})); });
}
struct D32 { bf16x8 c[2], s[2], n[2]; };
DI void d32_make(int l, int i, int sl, bool back, u32 base) {
  const int n = 16 * (i >> 1) + (l & 15);
  const float da = (float)(64 * n + (back ? sl : 0)) * (1.0f / 2048.0f);
  const float a0 = __builtin_fmaf(da, (float)(4 * (l >> 4)), (float)(back ? 0 : n * sl) * (1.0f / 2048.0f) - ((i & 1) ? 0.25f : 0.f));
  float cc[8];
  sfor<8>([&](auto E) { cc[E] = cosr(__builtin_fmaf(da, (float)(16 * (E >> 2) + (E & 3)), a0)); });
  *(LAS bf16x8*)(base + i * 1024 + l * 16) = pack8(cc);
}
DI void d32_read(D32& d, int l, u32 base) {
  sfor<2>([&](auto U) {
    d.c[U] = *(LAS bf16x8*)(base + 2 * U * 1024 + l * 16); d.s[U] = *(LAS bf16x8*)(base + (2 * U + 1) * 1024 + l * 16);
    d.n[U] = neg8(d.s[U]);
  });
}

constexpr u32 P_T = 32768, P_TP = 2064, P_V = 49280, P_VP = 464, P_D = 155648, P_X = 114688;
static_assert(P_T + 8 * P_TP <= P_V && P_V + 32 * P_VP + 64 <= 65536, "");
template <class F> DI void p1_third(Ctx& c, u32& epoch, F&& issue) {
  const int l = c.l, w = c.w, q = l >> 4, r = l & 15, sl = c.xcd * 8 + (c.idx & 7), c3 = c.idx >> 3;
  const rsrc_t rt = mkrs(&TABLES, T_END);
  v4u xv[5], sf[1][2], mf[2];
  const u32 xo = (64 * (4 * w + q) + sl) * 1280 + r * 16;
  sfor<5>([&](auto I) { BLD(xv[I], xo, c.rx, I * 256); });
  sfor<2>([&](auto I) { BLD(sf[0][I], l * 16, rt, T_S + c3 * 2048 + I * 1024); });
  sfor<2>([&](auto I) { BLD(mf[I], w < 7 ? l * 16 : OOB, rt, (7 * c3 + w) * 4096 + I * 1024); });
  __builtin_amdgcn_s_barrier();
  issue(IC(0));
  tag_wait(c, epoch);
  if (w < 4) d32_make(l, w, sl, false, P_D);
  u32 xw = P_X + w * 5120 + q * 1280 + r * 16, xr = P_X + w * 5120 + l * 8, xt = P_X + w * 5120 + 1024 + (l & 31) * 8;
  u32 tw = P_T + (r >> 1) * P_TP + (r & 1) * 512 + (w >> 2) * 1024 + (q >> 1) * 256 + (4 * (w & 3)) * 16 + (q & 1) * 8;
  u32 ta = P_T + w * P_TP + l * 16, va = P_V + r * P_VP + w * 64 + q * 16, v0 = P_V + (4 * w + q) * P_VP + r * 16;
  u32 z0 = WS_Z + sl * 1536 + c3 * 512 + (4 * w + q) * 98304 + r * 16;
  u32 so = (r == 0 && c3 == 0) ? WS_STAT + (64 * (4 * w + q) + sl) * 8 : OOB, k11 = 0x3f803f80u;
  float dc = (c3 == 0 ? r == 0 : (c3 == 2 && r == 12)) ? -40.0f : 0.f;
  OPAQUE(xw); OPAQUE(xr); OPAQUE(xt); OPAQUE(tw); OPAQUE(ta); OPAQUE(va); OPAQUE(v0); OPAQUE(z0); OPAQUE(so); PIN(dc);
  asm volatile("" : "+s"(k11));
  asm volatile("s_waitcnt vmcnt(4)" : "+v"(xv[0]), "+v"(xv[1]), "+v"(xv[2]), "+v"(xv[3]), "+v"(xv[4]), "+v"(sf[0][0]), "+v"(sf[0][1])::"memory");
  STAMP(1);
  sfor<5>([&](auto I) { *(LAS v4u*)(xw + I * 256) = xv[I]; });
  issue(IC(1));
  float s0 = 0.f, s1 = 0.f, q0 = 0.f, q1 = 0.f;
  sfor<10>([&](auto E) {
    const u32 a = xv[E / 2][2 * (E & 1)], b = xv[E / 2][2 * (E & 1) + 1];
    s0 = dot2(k11, a, s0); s1 = dot2(k11, b, s1); q0 = dot2(a, a, q0); q1 = dot2(b, b, q1);
  });
  s0 += s1; q0 += q1;
  GAP();
  issue(IC(2));
  f32x4 acc[4][1];
  sfor<4>([&](auto Z) { stage40<1>(xr + Z * 1280, xt + Z * 1280, sf, acc[Z]); });
  s0 += dpp<0xB1>(s0); q0 += dpp<0xB1>(q0); s0 += dpp<0x4E>(s0); q0 += dpp<0x4E>(q0);
  s0 += dpp<0x141>(s0); q0 += dpp<0x141>(q0); s0 += dpp<0x140>(s0); q0 += dpp<0x140>(q0);
  const float mean = s0 * (1.0f / 640.0f), var = __builtin_fmaxf(__builtin_fmaf(q0, 1.0f / 640.0f, -mean * mean), 0.f);
  const u32 stat = cvtpk(mean, __builtin_amdgcn_sqrtf(rbf(rbf(var) + 1e-5f)));
  const float rinv = __builtin_amdgcn_rcpf(bfhi(stat)), mr = bflo(stat) * rinv;
  sfor<4>([&](auto Z) {
    const float ri = __uint_as_float(__builtin_amdgcn_readlane(__float_as_uint(rinv), 16 * Z));
    const float mz = __uint_as_float(__builtin_amdgcn_readlane(__float_as_uint(mr), 16 * Z)) * dc;
    *(LAS v2u*)(tw + Z * 16) = pack4(acc[Z][0] * ri + mz);
  });
  issue(IC(3));
  asm volatile("s_waitcnt vmcnt(8) lgkmcnt(0)\n\ts_barrier" : "+v"(mf[0]), "+v"(mf[1])::"memory");
  if (w < 7) {
    const f32x4 z4 = {0.f, 0.f, 0.f, 0.f};
    D32 d;
    d32_read(d, l, P_D);
    const bf16x8 t0 = *(LAS bf16x8*)ta, t1 = *(LAS bf16x8*)(ta + 1024);
    const bf16x8 zr = pack44(mfma16(t0, asbf(mf[0]), z4), mfma16(t1, asbf(mf[0]), z4)), zi = pack44(mfma16(t0, asbf(mf[1]), z4), mfma16(t1, asbf(mf[1]), z4));
    sfor<2>([&](auto U) {
      *(LAS bf16x8*)(va + U * 16 * P_VP) = pack44(mfma16(zi, d.s[U], mfma16(zr, d.c[U], z4)), mfma16(zr, d.n[U], mfma16(zi, d.c[U], z4)));
    });
  }
  issue(IC(4));
  WAITBAR<63>();
  st128<SC1>(*(LAS v4u*)v0, c.rws, z0);
  st128<SC1>(*(LAS v4u*)(v0 + 256), c.rws, z0 + 256);
  st64<SC1>(v2u{stat, c.tag}, c.rws, so);
  CFENCE();
  STAMP(2);
}

constexpr int NXS = 4, NWS = 5, PRE = 5;
constexpr bool qIsX(int q) { return q % 3 == 0; }
constexpr int qIdx(int q) { return q % 3 == 0 ? q / 3 : 2 * (q / 3) + (q % 3) - 1; }
constexpr int qSlotOff(int q) { return qIsX(q) ? (qIdx(q) % NXS) * SLOTB : (NXS + qIdx(q) % NWS) * SLOTB; }
constexpr int qFree(int q) { return qIsX(q) ? (qIdx(q) < NXS ? 0 : 2 * (qIdx(q) - NXS) + 2) : (qIdx(q) < NWS ? 0 : qIdx(q) - NWS + 1); }
constexpr int needQ(int st) { return 3 * (st / 2) + 1 + (st & 1); }
constexpr int issuedBefore(int st) {
  int n = PRE;
  for (int s = 0; s < st; s++)
    for (int j = 0; j < 2; j++)
      if (n < 30 && qFree(n) <= s) n++;
  return n;
}
static_assert(issuedBefore(11) <= needQ(16), "the counter load is older than an entry that barrier 15 waits for");
template <int N> DI void WAITBARV() { asm volatile("s_waitcnt vmcnt(%0)\n\ts_barrier" ::"i"(N) : "memory"); }

constexpr int WDL = 20, WD0 = 16;
static_assert(issuedBefore(WD0) == 30, "no LDS-DMA is issued behind the register loads");
DI void glu_phase(Ctx& c, u32& epoch, v4u (&wd)[30]) {
  const int l = c.l, w = c.w, q = l >> 4, r = l & 15;
  const int panel = (c.xcd & 1) * 8 + (c.idx & 7), sub = c.idx >> 3, grp = (c.xcd >> 1) * 3 + sub;
#define ROT 2
  const int rot = ROT == 10 ? ((c.idx & 7) + 3 * sub) % 10 : (ROT == 2 ? 5 * ((c.idx + sub) & 1) : (ROT == 3 ? 3 * ((c.idx + sub) % 3) : 0));
  const rsrc_t rw = mkrs(w >= 4 ? c.wu : c.wg, 640 * 3072);
  u32 xvo[2];
  sfor<2>([&](auto I) { constexpr int i = I; xvo[i] = (u32)((panel * 128 + 8 * (2 * w + i) + (l >> 3)) * 1280 + (((l & 7) ^ ((4 * i + q) & 7)) * 16)); });
  const u32 wvo = (u32)((8 * (w & 3) + q) * 3072 + grp * 256 + ((r ^ (4 * q + 2 * (w & 1))) * 16));
  const u32 m0b = c.lb + w * 2048;
  auto dma = [&](auto Q, auto I) {
    constexpr int qq = Q, i = I;
    int blk = qq / 3 + rot;
    blk = blk >= 10 ? blk - 10 : blk;
    if constexpr (qIsX(qq)) DMA(m0b + qSlotOff(qq) + i * 1024, xvo[i], c.rx, (u32)(blk * 128));
    else DMA(m0b + qSlotOff(qq) + i * 1024, wvo, rw, (u32)((blk * 64 + (qIdx(qq) & 1) * 32 + i * 4) * 3072));
  };
  p1_third(c, epoch, [&](auto Q) { sfor<2>([&](auto I) { dma(Q, I); }); });
  if (w >= 4) __builtin_amdgcn_s_setprio(1);
  const int sh = w & 1, ch = w >> 1;
  u32 ta[3][2], xa[2];
  sfor<6>([&](auto Z) {
    constexpr int z = Z / 2, t = Z & 1;
    ta[z][t] = c.lb + z * 65536 + (u32)((8 * q + (r >> 2)) * 256 + (((2 * ch + t) ^ (2 * (r >> 2) + (q & 1))) * 32) + (l & 3) * 8);
    OPAQUE(ta[z][t]);
  });
  sfor<2>([&](auto M) { constexpr int m = M; xa[m] = c.lb + (u32)((sh * 64 + r) * 128 + (((4 * m + q) ^ (r >> 1)) * 16)); OPAQUE(xa[m]); });
  f32x4 acc[4][4];
  bf16x8 wf[2][4], xf[2][4];
  auto rdw = [&](auto ST, auto T) {
    constexpr int st = ST, t = T, off = (NXS + st % NWS) * SLOTB + (t >> 1) * 8192;
    wf[st & 1][t] = cat(trr(ta[off >> 16][t & 1] + (off & 65535)), trr(ta[off >> 16][t & 1] + (off & 65535) + 1024));
  };
  auto rdx = [&](auto ST, auto I) {
    constexpr int st = ST, i = I, off = ((st >> 1) % NXS) * SLOTB + i * 2048;
    xf[st & 1][i] = *(LAS bf16x8*)(xa[st & 1] + off);
  };
  WAITBAR<2 * (PRE - 1 - needQ(1)) + 3>();
  STAMP(3);
  sfor<4>([&](auto I) { rdw(IC(0), I); rdx(IC(0), I); });
  GAP();
  v2u sg[4], cg[4];
  u32 fw;
  auto side_loads = [&]() {
    sfor<4>([&](auto I) {
      sg[I] = ld64<SC1>(c.rws, WS_STAT + (panel * 128 + sh * 64 + I * 16 + r) * 8);
      cg[I] = ld64<SC1>(c.rws, WS_CS + ((I >> 1) * 1536 + grp * 128 + ch * 32 + (I & 1) * 16 + r) * 8);
    });
  };
  sfor<20>([&](auto ST) {
    constexpr int st = ST;
    if constexpr (st > 0 && st < 19) {
      constexpr int n = 2 * (issuedBefore(st) - 1 - needQ(st + 1)) + (st > WD0 ? 5 * (st - WD0) : 0);
      static_assert(n >= 0, "");
      WAITBARV<n>();
    }
    if constexpr (st == 1) {
      static_assert(needQ(2) == PRE - 1, "");
      st32<SC1>(c.tag, c.rws, threadIdx.x == 0 ? WS_FLAG + (F_Z + 64 * sub + c.xcd * 8 + (c.idx & 7)) * 128 : OOB);
      GAP();
    }
    if constexpr (st == WD0 - 5) BLD32(fw, w == 0 ? WS_FLAG + (F_W + l) * 128 : OOB, c.rws);
    if constexpr (st == WD0 - 1) {
      OPAQUE(fw);
      if (w == 0 && !__all(fw == c.tag)) poll_flags(c, F_W + l);
      GAP();
    }
    if constexpr (st == 19) WAITBAR<63>();
    constexpr int q0 = issuedBefore(st), q1 = issuedBefore(st + 1);
    sfor<16>([&](auto G) {
      constexpr int g = G, t = g >> 2, i = g & 3;
      acc[t][i] = mfma16(wf[st & 1][t], xf[st & 1][i], st == 0 ? f32x4{0.f, 0.f, 0.f, 0.f} : acc[t][i]);
      if constexpr (st + 1 < 20) {
        if constexpr (i == 3) rdw(IC(st + 1), IC(t));
        if constexpr (i == 1) rdx(IC(st + 1), IC(t));
      }
      if constexpr (i == 2 && q0 + (g >> 3) < q1) dma(IC(q0 + (g >> 3)), IC((g >> 2) & 1));
      if constexpr (st >= WD0 && g % 3 == 2) wd_load<5 * (st - WD0) + g / 3>(c, wd);
      if constexpr (st == 18 && g == 0) side_loads();
      GAP();
    });
  });
  STAMP(4);
  auto side_ok = [&]() {
    bool ok = true;
    sfor<4>([&](auto I) { ok = ok && sg[I].y == c.tag && cg[I].y == c.tag; });
    return __all(ok);
  };
  if (!side_ok()) {
#pragma nounroll
    for (u32 n = 0; n < SPIN_CAP; ++n) {
      __builtin_amdgcn_s_sleep(16);
      side_loads();
      if (side_ok()) break;
    }
  }
  CFENCE();
  __builtin_amdgcn_s_setprio(0);
  bf16x8 cf[4], mf[4];
  float r2[4];
  sfor<4>([&](auto T) {
    const float s0 = q == 0 ? __uint_as_float(cg[T].x) : 0.f;
    const float a0 = rbf(s0), a1 = rbf(s0 - a0);
    cf[T] = asbf(v4u{cvtpk(a0, a1), cvtpk(s0 - a0 - a1, 0.f), 0u, 0u});
    const u32 nm = (sg[T].x & 0xffffu) ^ 0x8000u, nm2 = nm | (nm << 16);
    mf[T] = asbf(v4u{nm2, nm2, 0u, 0u});
    const float ri = __builtin_amdgcn_rcpf(bfhi(sg[T].x));
    r2[T] = ri * ri;
  });
  sfor<4>([&](auto I) {
    constexpr int i = I;
    sfor<4>([&](auto T) { acc[T][i] = mfma16(cf[T], mf[i], acc[T][i]); });
    v4u o;
    sfor<4>([&](auto P) {
      constexpr int t = P >> 1, e = 2 * (P & 1);
      o[(int)P] = cvtpk(acc[t][i][e] * acc[t + 2][i][e] * r2[i], acc[t][i][e + 1] * acc[t + 2][i][e + 1] * r2[i]);
    });
    st128<SC1>(o, c.rws, l * 16, WS_H + ((panel * 8 + sh * 4 + i) * 48 + grp * 4 + ch) * 1024);
  });
  STAMP(5);
  __builtin_amdgcn_s_barrier();
  CFENCE();
  sfor<30 - WDL>([&](auto I) { wd_load<WDL + I>(c, wd); });
  STAMP(6);
  arrive_publish<30 - WDL>(c, C_ARRA, F_H + 12 * panel + grp);
  STAMP(7);
}

constexpr u32 S_W = 0, S_WP = 656, S_BP = 784, S_CP = 1568, S_U = 50176, S_UP = 1352;
constexpr u32 S_FI = 94208, S_DI = S_FI + 8192, S_CS = S_DI + 4096, S_SCR = S_CS + 2048;
static_assert(128 * S_WP <= S_FI && 32 * S_CP <= S_U && S_U + 32 * S_UP <= S_FI, "");
template <int NS> DI void p3_groups(const D32& d, const v4u (&mf)[3][2], const u32 (&va)[3], const u32 (&ua)[3]) {
  const f32x4 z4 = {0.f, 0.f, 0.f, 0.f};
  bf16x8 vr[NS], vi[NS];
  sfor<NS>([&](auto S) { vr[S] = cat(trr(va[S]), trr(va[S] + 16 * S_CP)); vi[S] = cat(trr(va[S] + 8), trr(va[S] + 16 * S_CP + 8)); });
  sfor<NS>([&](auto S) {
    sfor<2>([&](auto I) {
      const bf16x8 y = pack44(mfma16(vi[S], d.n[I], mfma16(vr[S], d.c[I], z4)), mfma16(vr[S], d.s[I], mfma16(vi[S], d.c[I], z4)));
      sfor<2>([&](auto C) { *(LAS v2u*)(ua[S] + I * 16 * S_UP + C * 32) = pack4(mfma16(asbf(mf[S][C]), y, z4)); });
    });
  });
}
struct F64 { bf16x8 c[2][2], s[2][2], n[2][2]; };
template <int NU> DI void p2_units(const F64& f, const f32x4 (&wr)[2][2][2], const f32x4 (&wi)[2][2][2], u32 ra, u32 wa) {
  const f32x4 z4 = {0.f, 0.f, 0.f, 0.f};
  bf16x8 vr[NU][2], vi[NU][2], pr[NU][2], pi[NU][2];
  sfor<2 * NU>([&](auto J) {
    constexpr int S = J >> 1, P = J & 1;
    const u32 ad = ra + S * 576 + P * S_BP;
    vr[S][P] = cat(trr(ad), trr(ad + 32 * S_BP)); vi[S][P] = cat(trr(ad + 8), trr(ad + 32 * S_BP + 8));
  });
  CFENCE();
  sfor<2 * NU>([&](auto J) {
    constexpr int S = J >> 1, P = J & 1;
    const f32x4 er = mfma16(f.s[P][0], vi[S][0], mfma16(f.c[P][0], vr[S][0], z4)), ei = mfma16(f.n[P][0], vr[S][0], mfma16(f.c[P][0], vi[S][0], z4));
    const f32x4 od = mfma16(f.s[P][1], vi[S][1], mfma16(f.c[P][1], vr[S][1], z4)), oi = mfma16(f.n[P][1], vr[S][1], mfma16(f.c[P][1], vi[S][1], z4));
    const f32x4 ar = er + od, ai = ei + oi, br = er - od, bi = ei - oi;
    pr[S][P] = pack44(ar * wr[S][P][0] - ai * wi[S][P][0], br * wr[S][P][1] - bi * wi[S][P][1]);
    pi[S][P] = pack44(ar * wi[S][P][0] + ai * wr[S][P][0], br * wi[S][P][1] + bi * wr[S][P][1]);
  });
  sfor<2 * NU>([&](auto J) {
    constexpr int S = J >> 1, P = J & 1;
    const f32x4 er = mfma16(pi[S][0], f.n[P][0], mfma16(pr[S][0], f.c[P][0], z4)), ei = mfma16(pr[S][0], f.s[P][0], mfma16(pi[S][0], f.c[P][0], z4));
    const u32 ad = wa + S * 576 + P * S_BP;
    const f32x4 od = mfma16(pi[S][1], f.n[P][1], mfma16(pr[S][1], f.c[P][1], z4)), oi = mfma16(pr[S][1], f.s[P][1], mfma16(pi[S][1], f.c[P][1], z4));
    *(LAS bf16x8*)ad = pack44(er + od, ei + oi);
    *(LAS bf16x8*)(ad + 32 * S_BP) = pack44(er - od, ei - oi);
  });
}

DI void spc_phase(Ctx& c, u32& epoch, v4u (&wd)[30]) {
  const int l = c.l, w = c.w, q = l >> 4, r = l & 15, qd = c.xcd >> 1, rr = l >> 2;
  const int si = c.xcd * 8 + (c.idx & 7), hf = si & 1;
  const rsrc_t rt = mkrs(&TABLES, T_END);
  v4u fv[12], dv[4];
  const rsrc_t rf = mkrs(w < 4 ? c.wr : c.wi, 2048 * 642);
  const u32 fo = ((si >> 1) + 32 * (16 * (w & 3) + q)) * 642 + r * 16, fo2 = r < 8 ? fo + 512 : (r == 8 ? fo + 498 : OOB), fo3 = fo2 + (r == 8 ? 4 : 0);
  auto f_load = [&](auto P) {
    constexpr u32 o = P * 128 * 642 - (P >> 1) * 4;
    fv[3 * P] = ld128(rf, fo, o); fv[3 * P + 1] = ld128(rf, fo + 256, o); fv[3 * P + 2] = ld128(rf, P < 2 ? fo2 : fo3, o);
  };
  DMA(S_FI + w * 1024, l * 16, rt, T_F + w * 1024);
  const int it = si * 8 + w, cj = (c.xcd & 1) * 8 + (c.idx & 7);
  if (hf) { f_load(IC(2)); f_load(IC(3)); f_load(IC(0)); f_load(IC(1)); }
  else { f_load(IC(0)); f_load(IC(1)); f_load(IC(2)); f_load(IC(3)); }
  {
    const u32 vo = it < 480 ? (u32)(((it / 10) * 32 + (l >> 3)) * 1280 + (it % 10) * 128 + (l & 7) * 16) : OOB;
    sfor<4>([&](auto I) { dv[I] = ld128(mkrs(c.wd, 1536 * 1280), vo, I * 8 * 1280); });
  }
  CFENCE();
  tag_wait(c, epoch);
  if (w < 4) d32_make(l, w, si, true, S_DI);
  sfor<12>([&](auto I) { if (I % 3 < 2 || r < 9) *(LAS v4u*)(S_W + (16 * w + 4 * (I / 3) + q) * S_WP + r * 16 + (I % 3) * 256) = fv[I]; });
  STAMP(3);
  WAITBAR<4>();
  STAMP(4);
  {
    v4u gd[4];
    tile_tr(c, dv, gd, S_SCR + w * 2048);
    sfor<4>([&](auto J) {
      const int c16 = (it % 10) * 4 + J;
      st128<SC1>(gd[J], c.rws, it < 480 ? WS_WD + (((c16 / 5) * 48 + it / 10) * 5 + c16 % 5) * 1024 + l * 16 : OOB);
    });
  }
  CFENCE();
  v4u cv[10];
  const rsrc_t rw = mkrs(cj >= 6 ? c.wu : c.wg, 640 * 3072);
  const u32 cvo = cj < 12 ? (u32)((8 * w + (l >> 3)) * 3072 + (384 * qd + 64 * (cj % 6)) * 2 + (l & 7) * 16) : OOB;
  sfor<5>([&](auto I) { cv[I] = ld128(rw, cvo, I * 64 * 3072); });
  CFENCE();
  const bool two = w < 3 - hf;
  f32x4 wr[2][2][2], wi[2][2][2];
  sfor<2>([&](auto S) {
    if (S == 0 || two) {
      const int kk = col_k(11 * hf + w + 8 * S, r);
      const float sc = ((kk == 0 || kk == 320) ? 1.0f : 2.0f) / (2048.0f * 640.0f);
      u32 ad = S_W + 8 * q * S_WP + 2 * kk + (kk >= 318 ? 14 : 4 * (q & 1)), ad2 = ad + 64 * S_WP;
      OPAQUE(ad2);
      sfor<16>([&](auto E) {
        constexpr int p = E >> 3, b = (E >> 2) & 1, e = E & 3, off = (2 * e + p + 32 * b) * S_WP;
        wr[S][p][b][e] = bflo(*(LAS u16*)(ad + off));
        wi[S][p][b][e] = bflo(*(LAS u16*)(ad2 + off));
      });
      sfor<4>([&](auto E) { wr[S][E >> 1][E & 1] *= sc; wi[S][E >> 1][E & 1] *= sc; PIN(wr[S][E >> 1][E & 1]); PIN(wi[S][E >> 1][E & 1]); });
    }
    if constexpr (S == 0) { CFENCE(); sfor<5>([&](auto I) { cv[5 + I] = ld128(rw, cvo, (5 + I) * 64 * 3072); }); CFENCE(); }
  });
  arrive_publish<10>(c, C_ARRW, F_W + si);
  F64 f;
  sfor<4>([&](auto P) {
    f.c[P >> 1][P & 1] = *(LAS bf16x8*)(S_FI + P * 1024 + l * 16);
    f.s[P >> 1][P & 1] = *(LAS bf16x8*)(S_FI + 4096 + P * 1024 + l * 16);
    f.n[P >> 1][P & 1] = neg8(f.s[P >> 1][P & 1]);
    PIN(f.n[P >> 1][P & 1]);
  });
  const int sl0 = w + (w >= (hf ? 3 : 7));
  u32 bb = S_W + (8 * w + q) * S_BP + r * 16, ra = S_W + sl0 * 64 + 2 * rr * S_BP + (l & 3) * 16, wa = S_W + sl0 * 64 + 2 * r * S_BP + q * 16;
  u32 zi = WS_Z + (si >> 1) * 98304 + hf * 768 + (8 * w + q) * 1536 + r * 16;
  u32 yo = WS_Y + (si >> 1) * 1536 + hf * 768 + (8 * w + q) * 49152 + r * 16;
  OPAQUE(bb); OPAQUE(ra); OPAQUE(wa); OPAQUE(zi); OPAQUE(yo);
  STAMP(5);
  {
    u32 k10 = 0x00003f80u, k01 = 0x3f800000u;
    asm volatile("" : "+s"(k10), "+s"(k01));
    float t[8] = {0.f, 0.f, 0.f, 0.f, 0.f, 0.f, 0.f, 0.f};
    sfor<40>([&](auto E) { constexpr int i = E / 4, e = E % 4; t[2 * e] = dot2(k10, cv[i][e], t[2 * e]); t[2 * e + 1] = dot2(k01, cv[i][e], t[2 * e + 1]); });
#pragma unroll
    for (int e = 0; e < 4; ++e) t[e] = fold32(t[e], t[e + 4]);
    t[0] = fold16(t[0], t[2]); t[1] = fold16(t[1], t[3]);
    const bool up = l & 8;
    *(LAS float*)(S_CS + w * 256 + (8 * (l & 7) + (l >> 3)) * 4) = (up ? t[1] : t[0]) + dpp<0x128>(up ? t[0] : t[1]);
  }
  STAMP(6);
  if (w >= 6) poll_flags(c, F_Z + 64 * (hf + w - 6) + l);
  WAITBAR<63>();
  STAMP(7);
  v4u sf[3][2], mf[3][2];
  {
    v4u y[6];
    sfor<6>([&](auto I) { y[I] = ld128<SC1>(c.rws, zi + (I % 3) * 256, (I / 3) * 4 * 1536); });
    GAP();
    float s = *(LAS float*)(S_CS + (l >> 3) * 256 + (w * 8 + (l & 7)) * 4);
    s += dpp<0x128>(s);
    s = fold16(s, s);
    s = fold32(s, s);
    st64<SC1>(v2u{__float_as_uint(s), c.tag}, c.rws, l < 8 && cj < 12 ? WS_CS + ((cj / 6) * 1536 + 384 * qd + 64 * (cj % 6) + w * 8 + l) * 8 : OOB);
    GAP();
    sfor<6>([&](auto I) { *(LAS v4u*)(bb + (I / 3) * 4 * S_BP + (I % 3) * 256) = y[I]; });
  }
  WAITBAR<63>();
  STAMP(8);
  if (two) p2_units<2>(f, wr, wi, ra, wa);
  else p2_units<1>(f, wr, wi, ra, wa);
  WAITBAR<63>();
  STAMP(9);
  sfor<6>([&](auto I) { st128<SC1>(*(LAS v4u*)(bb + (I / 3) * 4 * S_BP + (I % 3) * 256), c.rws, yo + (I % 3) * 256 + (I / 3) * 4 * 49152); });
  CFENCE();
  __builtin_amdgcn_s_barrier();
  sfor<6>([&](auto J) { mf[J >> 1][J & 1] = ld128(rt, (J < 4 || w < 5) ? l * 16 : OOB, (w + 8 * (J >> 1)) * 4096 + (2 + (J & 1)) * 1024); });
  WAITBAR<6>();
  if (w == 6) raise(c, F_Y + si);
  STAMP(10);
  sfor<6>([&](auto J) { sf[J >> 1][J & 1] = ld128(rt, l * 16, T_S + 6144 + J * 1024); });
  CFENCE();
  D32 d;
  d32_read(d, l, S_DI);
  PIN(d.n[0]); PIN(d.n[1]);
  u32 yi = WS_Y + si * 49152 + (4 * w + q) * 1536 + r * 16, cw = S_W + (4 * w + q) * S_CP + r * 16;
  u32 ur = S_U + 4 * w * S_UP + l * 8, ut = S_U + 4 * w * S_UP + 1024 + (l < 40 ? l : (l & 7) + 32) * 8;
  u32 ao = WS_A + (64 * (4 * w + (q & 1)) + si) * 1280 + r * 32 + (q >> 1) * 16, ao2 = r < 8 ? ao : OOB, ga[3], gb[3];
  sfor<3>([&](auto S) { const int g = w + 8 * S; ga[S] = S_W + rr * S_CP + (g + g / 7) * 64 + (l & 3) * 16; gb[S] = S_U + r * S_UP + g * 64 + q * 8; OPAQUE(ga[S]); OPAQUE(gb[S]); });
  OPAQUE(yi); OPAQUE(cw); OPAQUE(ur); OPAQUE(ut); OPAQUE(ao); OPAQUE(ao2);
  if (w == 7) poll_flags(c, F_Y + l);
  WAITBAR<63>();
  STAMP(11);
  {
    v4u y[6];
    sfor<6>([&](auto I) { y[I] = ld128<SC1>(c.rws, yi + I * 256); });
    GAP();
    __builtin_amdgcn_s_barrier();
    sfor<6>([&](auto I) { wd_load<I>(c, wd); });
    GAP();
    sfor<6>([&](auto I) { *(LAS v4u*)(cw + I * 256) = y[I]; });
  }
  WAITBAR<63>();
  STAMP(12);
  if (w < 5) p3_groups<3>(d, mf, ga, gb);
  else p3_groups<2>(d, mf, ga, gb);
  GAP();
  sfor<6>([&](auto I) { wd_load<6 + I>(c, wd); });
  WAITBAR<63>();
  STAMP(13);
  sfor<2>([&](auto Z) {
    f32x4 o[2][3];
    sfor<2>([&](auto Y) { stage40<3>(ur + (2 * Z + Y) * S_UP, ut + (2 * Z + Y) * S_UP, sf, o[Y]); });
    sfor<3>([&](auto T) {
      const v2u a = pack4(o[0][T]), b = pack4(o[1][T]);
      const auto x = __builtin_amdgcn_permlane16_swap(a.x, b.x, false, false), y = __builtin_amdgcn_permlane16_swap(a.y, b.y, false, false);
      st128<SC1>(v4u{x[0], y[0], x[1], y[1]}, c.rws, (T < 2 ? ao : ao2) + T * 512 + Z * 128 * 1280);
    });
  });
  WAITBAR<63>();
  STAMP(14);
  sfor<6>([&](auto I) { wd_load<12 + I>(c, wd); });
  arrive_publish<6>(c, C_ARRA, F_A + si);
  STAMP(15);
  sfor<12>([&](auto I) { wd_load<18 + I>(c, wd); });
  CFENCE();
}

constexpr u32 OUT_A = 0, OUT_B = 61440, OUT_S = 122880, OUT_O = OUT_S + 20480, OUT_OP = 176;
DI void out_phase(const Ctx& c, const v4u (&wd)[30], u32 epoch) {
  const int l = c.l, w = c.w, q = l >> 4, r = l & 15, a = w & 3;
  const int rt = 4 * c.xcd + (c.idx & 3), ct = c.idx >> 2;
  constexpr int HW = 2;
  u32 ho[4];
  sfor<4>([&](auto I) { ho[I] = WS_H + ((rt * 4 + ((a + I) & 3)) * 48 + 6 * out_kw(c)) * 1024; });
  v4u hq[HW][4];
  flag_wait(c, w == 0, C_GOH, F_H + 12 * (rt >> 1) + (l < 12 ? l : 0));
  STAMP(16);
  if (w < 4) __builtin_amdgcn_s_setprio(1);
  sfor<HW>([&](auto I) { sfor<4>([&](auto ST) { hq[I][ST] = ld128<SC1>(c.rws, l * 16, ho[ST] + I * 1024); }); });
  GAP();
  f32x4 acc[5][4];
  sfor<6>([&](auto I) {
    constexpr int i = I;
    sfor<4>([&](auto ST) {
      sfor<5>([&](auto T) { acc[T][ST] = mfma16(asbf(wd[5 * i + T]), asbf(hq[i % HW][ST]), i == 0 ? f32x4{0.f, 0.f, 0.f, 0.f} : acc[T][ST]); });
      if constexpr (i + HW < 6) hq[i % HW][ST] = ld128<SC1>(c.rws, l * 16, ho[ST] + (i + HW) * 1024);
      GAP();
    });
  });
  STAMP(17);
  __builtin_amdgcn_s_setprio(0);
  const u32 pb = (w < 4 ? OUT_A : OUT_B) + l * 16;
  sfor<3>([&](auto I) { sfor<5>([&](auto T) { *(LAS f32x4*)(pb + (((a + I + 1) & 3) * 3 + I) * 5120 + T * 1024) = acc[T][I + 1]; }); });
  if (w < 4) {
    if (w == 0) poll_flags(c, F_A + l);
    group_barrier(c, C_OUTA, 4);
    STAMP(18);
    v4u xs[3], as[3];
    u32 vo[3], lo[3];
    sfor<3>([&](auto I) {
      const u32 ch = 64 * I + l, row = (ch * 6554u) >> 16, cc = ch - 10 * row;
      vo[I] = ch < 160 ? (rt * 64 + a * 16 + row) * 1280 + ct * 160 + cc * 16 : OOB;
      lo[I] = OUT_O + (a * 16 + (row & 15)) * OUT_OP + cc * 16;
      xs[I] = ld128(c.rx, vo[I]); as[I] = ld128<SC1>(c.rws, vo[I], WS_A);
    });
    GAP();
    sfor<5>([&](auto T) {
      f32x4 s = acc[T][0];
      sfor<3>([&](auto J) { s += *(LAS f32x4*)(pb + (a * 3 + J) * 5120 + T * 1024); });
      *(LAS f32x4*)(OUT_S + (a * 5 + T) * 1024 + l * 16) = s;
    });
    STAMP(19);
    WAITBAR<63>();
    sfor<12>([&](auto E) { constexpr int i = E / 4, e = E % 4; xs[i][e] = cvtpk(bflo(xs[i][e]) + bflo(as[i][e]), bfhi(xs[i][e]) + bfhi(as[i][e])); });
    sfor<3>([&](auto I) { PIN(xs[I]); });
    WAITBAR<63>();
    STAMP(20);
    const rsrc_t ro = mkrs(c.out, 2048 * 1280);
    sfor<3>([&](auto I) {
      const v4u b = *(LAS v4u*)lo[I];
      v4u o;
      sfor<4>([&](auto E) { o[(int)E] = cvtpk(bflo(xs[I][(int)E]) + bflo(b[(int)E]), bfhi(xs[I][(int)E]) + bfhi(b[(int)E])); });
      st128(o, ro, vo[I]);
    });
  } else {
    STAMP(19);
    WAITBAR<63>();
    sfor<5>([&](auto T) {
      f32x4 s = acc[T][0] + *(LAS f32x4*)(OUT_S + (a * 5 + T) * 1024 + l * 16);
      sfor<3>([&](auto J) { s += *(LAS f32x4*)(pb + (a * 3 + J) * 5120 + T * 1024); });
      *(LAS v2u*)(OUT_O + (a * 16 + r) * OUT_OP + T * 32 + q * 8) = pack4(s);
    });
    WAITBAR<63>();
    if (w == 4 && blockIdx.x == 0) {
      if (l == 0) st32<SC1>(epoch, c.rws, 0);
    }
  }
}

__global__ __launch_bounds__(512, 2) void k_all(const u16* x, const u16* wr, const u16* wi, const u16* wg, const u16* wu, const u16* wd, u16* out, char* ws) {
  Ctx c;
  c.ws = ws;
  c.rx = mkrs(x, 2048 * 1280);
  c.rws = mkrs(ws, WS_END);
  c.wr = wr; c.wi = wi; c.wg = wg; c.wu = wu; c.wd = wd; c.out = out;
  c.l = threadIdx.x & 63;
  c.w = __builtin_amdgcn_readfirstlane(threadIdx.x >> 6);
  c.xcd = blockIdx.x & 7;
  c.idx = blockIdx.x >> 3;
  u32 epoch;
  asm volatile("s_load_dword %0, %1, 0x0" : "=s"(epoch) : "s"(ws) : "memory");
  if (threadIdx.x < C_N) *(LAS u32*)(c.lb + L_CTL + threadIdx.x * 4) = 0;
  STAMP(0);
  const bool spc = (c.idx >> 3) == 3;
  v4u wdf[30];
  if (spc) spc_phase(c, epoch, wdf);
  else glu_phase(c, epoch, wdf);
  GAP();
  out_phase(c, wdf, epoch);
  STAMP(21);
}

extern "C" void kernel_launch(void* const* d_in, const int* in_sizes, int n_in, void* d_out, int out_size, void* d_ws, size_t ws_size, hipStream_t stream) {
  (void)hipFuncSetAttribute((const void*)k_all, hipFuncAttributeMaxDynamicSharedMemorySize, LDS_BYTES);
  k_all<<<256, 512, LDS_BYTES, stream>>>((const u16*)d_in[0], (const u16*)d_in[1], (const u16*)d_in[2], (const u16*)d_in[4], (const u16*)d_in[5],
                                         (const u16*)d_in[6], (u16*)d_out, (char*)d_ws);
}
```

```cpp
#define PROBE 0
#define PROBE_WG 100
#define PROBE_W 0
#include <hip/hip_runtime.h>
#include <utility>

typedef unsigned u32;
typedef unsigned short u16;
typedef unsigned long long u64;
typedef unsigned v4u __attribute__((ext_vector_type(4)));
typedef unsigned v2u __attribute__((ext_vector_type(2)));
typedef float f32x2 __attribute__((ext_vector_type(2)));
typedef float f32x4 __attribute__((ext_vector_type(4)));
typedef __bf16 bf16x2 __attribute__((ext_vector_type(2)));
typedef __bf16 bf16x4 __attribute__((ext_vector_type(4)));
typedef __bf16 bf16x8 __attribute__((ext_vector_type(8)));
typedef __amdgpu_buffer_rsrc_t rsrc_t;
#define LAS __attribute__((address_space(3)))
#define GAS __attribute__((address_space(1)))
#define DI __device__ __forceinline__
#define GAP() __builtin_amdgcn_sched_barrier(0)
#define IC(x) std::integral_constant<int, (x)>{}
#pragma clang diagnostic ignored "-Wint-to-pointer-cast"

template <class F, int... Is> DI void sfor_(F&& f, std::integer_sequence<int, Is...>) { (f(std::integral_constant<int, Is>{}), ...); }
template <int N, class F> DI void sfor(F&& f) { sfor_(f, std::make_integer_sequence<int, N>{}); }

constexpr u32 WS_STAT = 0x4000;
constexpr u32 WS_CS = 0x8000;
constexpr u32 WS_FLAG = 0x20000, F_Z = 0, F_H = 192, F_W = 384, F_Y = 448, F_A = 512;
constexpr u32 WS_Z = 0x40000;
constexpr u32 WS_Y = WS_Z + 0x300000;
constexpr u32 WS_WD = WS_Y + 0x300000;
constexpr u32 WS_H = WS_WD + 0x1E0000;
constexpr u32 WS_A = WS_H + 0x600000;
constexpr u32 WS_END = WS_A + 0x280000;
constexpr u32 OOB = 0x40000000u;
constexpr int SC1 = 16;
constexpr u32 SPIN_CAP = 1u << 16;
constexpr u32 LDS_BYTES = 163840, SLOTB = 16384;
constexpr u32 L_CTL = LDS_BYTES - 256;
enum { C_ARRZ = 0, C_ARRW = 1, C_ARRY = 3, C_ARRA = 4, C_GOZ = 5, C_GOY = 6, C_GOH = 7, C_GOA = 8, C_OUTA = 9, C_N = 16 };

DI u32 cvtpk(float lo, float hi) { f32x2 v = {lo, hi}; return __builtin_bit_cast(u32, __builtin_convertvector(v, bf16x2)); }
DI float bflo(u32 v) { return __uint_as_float(v << 16); }
DI float bfhi(u32 v) { return __uint_as_float(v & 0xffff0000u); }
DI float rbf(float f) { return bflo(cvtpk(f, f)); }
DI bf16x8 asbf(v4u v) { return __builtin_bit_cast(bf16x8, v); }
DI v4u asu(bf16x8 v) { return __builtin_bit_cast(v4u, v); }
DI rsrc_t mkrs(const void* p, u32 n) { return __builtin_amdgcn_make_buffer_rsrc((void*)p, 0, n, 0x00020000); }
DI void DMA(u32 m0v, u32 voff, rsrc_t rs, u32 soff) {
  asm volatile("s_mov_b32 m0, %0\n\ts_nop 0\n\tbuffer_load_dwordx4 %1, %2, %3 offen lds" ::"s"(m0v), "v"(voff), "s"(rs), "s"(soff) : "memory");
}
DI void BLD(v4u& d, u32 voff, rsrc_t rs, u32 soff) { asm volatile("buffer_load_dwordx4 %0, %1, %2, %3 offen" : "=v"(d) : "v"(voff), "s"(rs), "s"(soff) : "memory"); }
DI void BLD32(u32& d, u32 voff, rsrc_t rs) { asm volatile("buffer_load_dword %0, %1, %2, 0 offen sc1" : "=v"(d) : "v"(voff), "s"(rs) : "memory"); }
template <int N> DI void WAITBAR() { asm volatile("s_waitcnt vmcnt(%0) lgkmcnt(0)\n\ts_barrier" ::"i"(N) : "memory"); }
DI void DRAIN() { asm volatile("s_waitcnt vmcnt(0)" ::: "memory"); }
DI void OPAQUE(u32& v) { asm volatile("" : "+v"(v)); }
DI void PIN(bf16x8& v) { asm volatile("" : "+v"(v)); }
DI void PIN(v4u& v) { asm volatile("" : "+v"(v)); }
DI void PIN(f32x4& v) { asm volatile("" : "+v"(v)); }
DI void PIN(float& v) { asm volatile("" : "+v"(v)); }
DI void CFENCE() { asm volatile("" ::: "memory"); }
DI bf16x4 trr(u32 a) { return __builtin_amdgcn_ds_read_tr16_b64_v4bf16((LAS bf16x4*)a); }
DI bf16x8 cat(bf16x4 a, bf16x4 b) { return __builtin_shufflevector(a, b, 0, 1, 2, 3, 4, 5, 6, 7); }
DI f32x4 mfma16(bf16x8 a, bf16x8 b, f32x4 c) { return __builtin_amdgcn_mfma_f32_16x16x32_bf16(a, b, c, 0, 0, 0); }
template <int AUX = 0> DI v4u ld128(rsrc_t rs, u32 voff, u32 soff = 0) { return __builtin_amdgcn_raw_buffer_load_b128(rs, voff, soff, AUX); }
template <int AUX = 0> DI v2u ld64(rsrc_t rs, u32 voff, u32 soff = 0) { return __builtin_amdgcn_raw_buffer_load_b64(rs, voff, soff, AUX); }
template <int AUX = 0> DI u32 ld32(rsrc_t rs, u32 voff, u32 soff = 0) { return __builtin_amdgcn_raw_buffer_load_b32(rs, voff, soff, AUX); }
template <int AUX = 0> DI void st128(v4u v, rsrc_t rs, u32 voff, u32 soff = 0) { __builtin_amdgcn_raw_buffer_store_b128(v, rs, voff + soff, 0, AUX); }
template <int AUX = 0> DI void st64(v2u v, rsrc_t rs, u32 voff, u32 soff = 0) { __builtin_amdgcn_raw_buffer_store_b64(v, rs, voff, soff, AUX); }
template <int AUX = 0> DI void st32(u32 v, rsrc_t rs, u32 voff, u32 soff = 0) { __builtin_amdgcn_raw_buffer_store_b32(v, rs, voff, soff, AUX); }
DI float cosr(float rev) { return __builtin_amdgcn_cosf(rev); }
DI float sinr(float rev) { return __builtin_amdgcn_sinf(rev); }
DI float rev640(int p) {
  const float pf = (float)p;
  return __builtin_fmaf(__builtin_floorf(pf * (1.0f / 640.0f)), -640.0f, pf) * (1.0f / 640.0f);
}
template <int CTRL> DI float dpp(float v) { return __int_as_float(__builtin_amdgcn_update_dpp(0, __float_as_int(v), CTRL, 0xf, 0xf, true)); }
DI float fold32(float a, float b) {
  const auto r = __builtin_amdgcn_permlane32_swap(__float_as_uint(a), __float_as_uint(b), false, false);
  return __uint_as_float(r[0]) + __uint_as_float(r[1]);
}
DI float fold16(float a, float b) {
  const auto r = __builtin_amdgcn_permlane16_swap(__float_as_uint(a), __float_as_uint(b), false, false);
  return __uint_as_float(r[0]) + __uint_as_float(r[1]);
}
DI float wave_sum(float v) {
  v += dpp<0xB1>(v); v += dpp<0x4E>(v); v += dpp<0x141>(v); v += dpp<0x140>(v);
  v = fold16(v, v);
  return fold32(v, v);
}

#define STAMP(i) do { if ((i) == 0 || (i) == PROBE) { GAP(); const u32 t_ = (u32)__builtin_amdgcn_s_memrealtime(); if (c.l == 0) *(volatile LAS u32*)(L_CTL + 128 + c.w * 8 + ((i) ? 4 : 0)) = t_; GAP(); } } while (0)
struct Ctx {
  char* ws;
  rsrc_t rx, rws;
  const u16 *wr, *wi, *wg, *wu, *wd;
  u16* out;
  static constexpr u32 lb = 0;
  u32 tag;
  int l, w, xcd, idx;
};

DI u32 lds_ld(u32 a) { return __hip_atomic_load((LAS u32*)a, __ATOMIC_RELAXED, __HIP_MEMORY_SCOPE_WORKGROUP); }
DI void group_barrier(const Ctx& c, int word, u32 target) {
  asm volatile("s_waitcnt lgkmcnt(0)" ::: "memory");
  if (c.l == 0) __hip_atomic_fetch_add((LAS u32*)(c.lb + L_CTL + word * 4), 1u, __ATOMIC_RELAXED, __HIP_MEMORY_SCOPE_WORKGROUP);
#pragma nounroll
  for (u32 n = 0; n < (SPIN_CAP << 8) && lds_ld(c.lb + L_CTL + word * 4) < target; ++n) __builtin_amdgcn_s_sleep(1);
  CFENCE();
}
DI void raise(const Ctx& c, u32 flag) { if (c.l == 0) st32<SC1>(c.tag, c.rws, WS_FLAG + flag * 128); }
template <int N = 0> DI void arrive_publish(const Ctx& c, int word, u32 flag) {
  asm volatile("s_waitcnt vmcnt(%0)" ::"i"(N) : "memory");
  u32 old = 0;
  if (c.l == 0) old = __hip_atomic_fetch_add((LAS u32*)(L_CTL + word * 4), 1u, __ATOMIC_RELAXED, __HIP_MEMORY_SCOPE_WORKGROUP);
  old = __builtin_amdgcn_readfirstlane(old);
  if (old == 7u) raise(c, flag);
}
DI void poll_flags(const Ctx& c, u32 flag) {
#pragma nounroll
  for (u32 i = 0; i < SPIN_CAP && ld32<SC1>(c.rws, WS_FLAG + flag * 128) != c.tag; ++i) __builtin_amdgcn_s_sleep(2);
  CFENCE();
}
DI void flag_wait(const Ctx& c, bool leader, int word, u32 flag) {
  if (leader) {
    poll_flags(c, flag);
    if (c.l == 0) __hip_atomic_store((LAS u32*)(L_CTL + word * 4), 1u, __ATOMIC_RELAXED, __HIP_MEMORY_SCOPE_WORKGROUP);
  } else {
#pragma nounroll
    for (u32 i = 0; i < (SPIN_CAP << 6) && lds_ld(L_CTL + word * 4) == 0u; ++i) __builtin_amdgcn_s_sleep(1);
    CFENCE();
  }
}
DI void tag_wait(Ctx& c, u32 raw, u32& epoch) {
  asm volatile("s_waitcnt lgkmcnt(0)\n\ts_add_u32 %0, %1, 1" : "=s"(epoch) : "s"(raw) : "scc");
  c.tag = (epoch * 0x9E3779B1u) ^ 0x5bd1e995u;
}
DI int out_kw(const Ctx& c) { return (c.w + (c.idx >> 2) + 2 * (c.idx & 3)) & 7; }
template <int I> DI void wd_load(const Ctx& c, v4u (&wd)[30]) {
  wd[I] = ld128<SC1>(c.rws, c.l * 16 + (I % 4) * 1024, WS_WD + ((c.idx >> 2) * 48 + 6 * out_kw(c)) * 5120 + (I / 4) * 4096);
}

DI bf16x8 pack8(const float (&v)[8]) { return asbf(v4u{cvtpk(v[0], v[1]), cvtpk(v[2], v[3]), cvtpk(v[4], v[5]), cvtpk(v[6], v[7])}); }
DI bf16x8 pack44(f32x4 a, f32x4 b) { return asbf(v4u{cvtpk(a[0], a[1]), cvtpk(a[2], a[3]), cvtpk(b[0], b[1]), cvtpk(b[2], b[3])}); }
DI v2u pack4(f32x4 a) { return v2u{cvtpk(a[0], a[1]), cvtpk(a[2], a[3])}; }
DI bf16x8 neg8(bf16x8 v) { return asbf(asu(v) ^ 0x80008000u); }
DI float dot2(u32 a, u32 b, float c) { return __builtin_amdgcn_fdot2_f32_bf16(__builtin_bit_cast(bf16x2, a), __builtin_bit_cast(bf16x2, b), c, false); }
DI bf16x8 cos8(float a0, float da) {
  float cc[8];
  sfor<8>([&](auto E) { cc[E] = cosr(__builtin_fmaf(da, (float)E, a0)); });
  return pack8(cc);
}
DI void tile_tr(const Ctx& c, const v4u (&v)[4], v4u (&f)[4], u32 buf) {
  const int l = c.l, rr = 4 * (l >> 4) + ((l & 15) >> 2);
  const u32 wa = buf + (l >> 3) * 128 + (((l & 7) ^ (((l >> 4) & 3) << 1)) * 16), ra = buf + rr * 128 + (l & 3) * 8;
  bf16x4 t[2][4];
#pragma unroll
  for (int u = 0; u < 2; ++u) {
    *(LAS v4u*)wa = v[2 * u];
    *(LAS v4u*)(wa + 1024) = v[2 * u + 1];
    CFENCE();
#pragma unroll
    for (int j = 0; j < 4; ++j) t[u][j] = trr(ra + ((j ^ ((rr >> 1) & 3)) * 32));
    CFENCE();
  }
#pragma unroll
  for (int j = 0; j < 4; ++j) f[j] = asu(cat(t[0][j], t[1][j]));
}
struct Cos640 { u16 v[640]; };
constexpr Cos640 make_cos() {
  Cos640 t{};
  for (int i = 0; i < 640; ++i) {
    int n = i > 320 ? 640 - i : i;
    const bool neg = n > 160;
    if (neg) n = 320 - n;
    const double x = 6.283185307179586476925 * n / 640.0, x2 = x * x;
    double term = 1.0, sum = 1.0;
    for (int j = 1; j <= 12; ++j) { term *= -x2 / ((2 * j - 1) * (2 * j)); sum += term; }
    const float f = n == 160 ? 0.f : (float)(neg ? -sum : sum);
    const u32 u = __builtin_bit_cast(u32, f);
    t.v[i] = (u16)((u + 0x7fffu + ((u >> 16) & 1u)) >> 16);
  }
  return t;
}
constexpr Cos640 COS = make_cos();
constexpr int col_kc(int g, int m) { return g == 20 ? 320 : (g == 0 ? (m < 8 ? 40 * m : 40 * m - 300) : (g + 40 * m > 320 ? 640 - g - 40 * m : g + 40 * m)); }
constexpr u16 m_at(int g, int cc, int m, int cp, int d2) {
  const bool edge = g == 0 || g == 20, on = !edge || (g == 0 ? (m >> 3) == cp : (m == 0 && cp == 0));
  return on ? COS.v[(col_kc(g, m) * d2 + 160 * cc + ((cp && !edge) ? (g + 40 * m > 320 ? 160 : 480) : 0)) % 640] : (u16)0;
}
struct MGroup { u16 v[4][512]; };
constexpr MGroup make_m(int g) {
  MGroup t{};
  for (int h = 0; h < 2; ++h)
    for (int l = 0; l < 64; ++l)
      for (int e = 0; e < 8; ++e) {
        t.v[h][l * 8 + e] = m_at(g, h, l & 15, l >> 5, 8 * ((l >> 4) & 1) + e);
        t.v[2 + h][l * 8 + e] = m_at(g, e >> 2, 4 * (l >> 4) + (e & 3), h, l & 15);
      }
  return t;
}
template <int G> constexpr MGroup M_OF = make_m(G);
struct Small { u16 s[6][1024]; u16 f[8][512]; };
constexpr Small make_small() {
  Small t{};
  for (int w = 0; w < 6; ++w)
    for (int l = 0; l < 64; ++l)
      for (int e = 0; e < 12; ++e) {
        const int k = 16 * (e >> 2) + 4 * (l >> 4) + (e & 3), j = w < 3 ? 14 * w + (l & 15) : k, d1 = w < 3 ? k : 16 * (w - 3) + (l & 15);
        const int g = j == 1 ? 20 : (j == 40 ? 0 : j >> 1);
        t.s[w][(e >> 3) * 512 + l * 8 + (e & 7)] = (j <= 40 && d1 < 40) ? COS.v[(16 * g * d1 + (((j & 1) && j != 1) ? 160 : 0)) % 640] : (u16)0;
      }
  for (int w = 0; w < 8; ++w)
    for (int l = 0; l < 64; ++l)
      for (int e = 0; e < 8; ++e)
        t.f[w][l * 8 + e] = COS.v[(10 * (2 * (l & 15) + ((w >> 1) & 1)) * (2 * (4 * (l >> 4) + (e & 3)) + (w & 1) + 32 * (e >> 2)) + (w >= 4 ? 480 : 0)) % 640];
  return t;
}
constexpr Small SMALL = make_small();
struct Tables { MGroup m[21]; Small s; };
template <int... G> constexpr Tables make_tables(std::integer_sequence<int, G...>) { return Tables{{M_OF<G>...}, SMALL}; }
static __device__ __attribute__((aligned(128))) const Tables TABLES = make_tables(std::make_integer_sequence<int, 21>{});
constexpr u32 T_S = 21 * 4096, T_F = T_S + 12288, T_END = T_F + 8192;
static_assert(sizeof(Tables) == T_END, "");

DI int col_k(int g, int m) {
  const int kp = g + 40 * m;
  return g == 20 ? 320 : (g == 0 ? (m < 8 ? kp : kp - 300) : (kp > 320 ? 640 - kp : kp));
}
template <int NT> DI void stage40(u32 a, u32 tail, const v4u (&f)[NT][2], f32x4 (&acc)[NT]) {
  const bf16x8 x0 = cat(trr(a), trr(a + 512)), x1 = cat(trr(tail), trr(tail));
  sfor<NT>([&](auto T) { acc[T] = mfma16(x1, asbf(f[T][1]), mfma16(x0, asbf(f[T][0]), f32x4{0.f, 0.f, 0.f, 0.f})); });
}
struct D32 { bf16x8 c[2], s[2], n[2]; };
DI void d32_make(int l, int i, int sl, bool back, u32 base) {
  const int n = 16 * (i >> 1) + (l & 15);
  const float da = (float)(64 * n + (back ? sl : 0)) * (1.0f / 2048.0f);
  const float a0 = __builtin_fmaf(da, (float)(4 * (l >> 4)), (float)(back ? 0 : n * sl) * (1.0f / 2048.0f) - ((i & 1) ? 0.25f : 0.f));
  float cc[8];
  sfor<8>([&](auto E) { cc[E] = cosr(__builtin_fmaf(da, (float)(16 * (E >> 2) + (E & 3)), a0)); });
  *(LAS bf16x8*)(base + i * 1024 + l * 16) = pack8(cc);
}
DI void d32_read(D32& d, int l, u32 base) {
  sfor<2>([&](auto U) {
    d.c[U] = *(LAS bf16x8*)(base + 2 * U * 1024 + l * 16); d.s[U] = *(LAS bf16x8*)(base + (2 * U + 1) * 1024 + l * 16);
    d.n[U] = neg8(d.s[U]);
  });
}

constexpr u32 P_T = 32768, P_TP = 2064, P_V = 49280, P_VP = 464, P_D = 155648, P_X = 114688;
static_assert(P_T + 8 * P_TP <= P_V && P_V + 32 * P_VP + 64 <= 65536, "");
template <class F> DI void p1_third(Ctx& c, u32 raw, u32& epoch, F&& issue) {
  const int l = c.l, w = c.w, q = l >> 4, r = l & 15, sl = c.xcd * 8 + (c.idx & 7), c3 = c.idx >> 3;
  const rsrc_t rt = mkrs(&TABLES, T_END);
  v4u xv[5], sf[1][2], mf[2];
  const u32 xo = (64 * (4 * w + q) + sl) * 1280 + r * 16;
  sfor<5>([&](auto I) { BLD(xv[I], xo, c.rx, I * 256); });
  sfor<2>([&](auto I) { BLD(sf[0][I], l * 16, rt, T_S + c3 * 2048 + I * 1024); });
  sfor<2>([&](auto I) { BLD(mf[I], w < 7 ? l * 16 : OOB, rt, (7 * c3 + w) * 4096 + I * 1024); });
  __builtin_amdgcn_s_barrier();
  issue(IC(0));
  tag_wait(c, raw, epoch);
  if (w < 4) d32_make(l, w, sl, false, P_D);
  u32 xw = P_X + w * 5120 + q * 1280 + r * 16, xr = P_X + w * 5120 + l * 8, xt = P_X + w * 5120 + 1024 + (l & 31) * 8;
  u32 tw = P_T + (r >> 1) * P_TP + (r & 1) * 512 + (w >> 2) * 1024 + (q >> 1) * 256 + (4 * (w & 3)) * 16 + (q & 1) * 8;
  u32 ta = P_T + w * P_TP + l * 16, va = P_V + r * P_VP + w * 64 + q * 16, v0 = P_V + (4 * w + q) * P_VP + r * 16;
  u32 z0 = WS_Z + sl * 1536 + c3 * 512 + (4 * w + q) * 98304 + r * 16;
  u32 so = (r == 0 && c3 == 0) ? WS_STAT + (64 * (4 * w + q) + sl) * 8 : OOB, k11 = 0x3f803f80u;
  float dc = (c3 == 0 ? r == 0 : (c3 == 2 && r == 12)) ? -40.0f : 0.f;
  OPAQUE(xw); OPAQUE(xr); OPAQUE(xt); OPAQUE(tw); OPAQUE(ta); OPAQUE(va); OPAQUE(v0); OPAQUE(z0); OPAQUE(so); PIN(dc);
  asm volatile("" : "+s"(k11));
  asm volatile("s_waitcnt vmcnt(4)" : "+v"(xv[0]), "+v"(xv[1]), "+v"(xv[2]), "+v"(xv[3]), "+v"(xv[4]), "+v"(sf[0][0]), "+v"(sf[0][1])::"memory");
  STAMP(1);
  sfor<5>([&](auto I) { *(LAS v4u*)(xw + I * 256) = xv[I]; });
  issue(IC(1));
  float s0 = 0.f, s1 = 0.f, q0 = 0.f, q1 = 0.f;
  sfor<10>([&](auto E) {
    const u32 a = xv[E / 2][2 * (E & 1)], b = xv[E / 2][2 * (E & 1) + 1];
    s0 = dot2(k11, a, s0); s1 = dot2(k11, b, s1); q0 = dot2(a, a, q0); q1 = dot2(b, b, q1);
  });
  s0 += s1; q0 += q1;
  GAP();
  issue(IC(2));
  f32x4 acc[4][1];
  sfor<4>([&](auto Z) { stage40<1>(xr + Z * 1280, xt + Z * 1280, sf, acc[Z]); });
  s0 += dpp<0xB1>(s0); q0 += dpp<0xB1>(q0); s0 += dpp<0x4E>(s0); q0 += dpp<0x4E>(q0);
  s0 += dpp<0x141>(s0); q0 += dpp<0x141>(q0); s0 += dpp<0x140>(s0); q0 += dpp<0x140>(q0);
  const float mean = s0 * (1.0f / 640.0f), var = __builtin_fmaxf(__builtin_fmaf(q0, 1.0f / 640.0f, -mean * mean), 0.f);
  const u32 stat = cvtpk(mean, __builtin_amdgcn_sqrtf(rbf(rbf(var) + 1e-5f)));
  const float rinv = __builtin_amdgcn_rcpf(bfhi(stat)), mr = bflo(stat) * rinv;
  sfor<4>([&](auto Z) {
    const float ri = __uint_as_float(__builtin_amdgcn_readlane(__float_as_uint(rinv), 16 * Z));
    const float mz = __uint_as_float(__builtin_amdgcn_readlane(__float_as_uint(mr), 16 * Z)) * dc;
    *(LAS v2u*)(tw + Z * 16) = pack4(acc[Z][0] * ri + mz);
  });
  issue(IC(3));
  asm volatile("s_waitcnt vmcnt(8) lgkmcnt(0)\n\ts_barrier" : "+v"(mf[0]), "+v"(mf[1])::"memory");
  if (w < 7) {
    const f32x4 z4 = {0.f, 0.f, 0.f, 0.f};
    D32 d;
    d32_read(d, l, P_D);
    const bf16x8 t0 = *(LAS bf16x8*)ta, t1 = *(LAS bf16x8*)(ta + 1024);
    const bf16x8 zr = pack44(mfma16(t0, asbf(mf[0]), z4), mfma16(t1, asbf(mf[0]), z4)), zi = pack44(mfma16(t0, asbf(mf[1]), z4), mfma16(t1, asbf(mf[1]), z4));
    sfor<2>([&](auto U) {
      *(LAS bf16x8*)(va + U * 16 * P_VP) = pack44(mfma16(zi, d.s[U], mfma16(zr, d.c[U], z4)), mfma16(zr, d.n[U], mfma16(zi, d.c[U], z4)));
    });
  }
  issue(IC(4));
  WAITBAR<63>();
  st128<SC1>(*(LAS v4u*)v0, c.rws, z0);
  st128<SC1>(*(LAS v4u*)(v0 + 256), c.rws, z0 + 256);
  st64<SC1>(v2u{stat, c.tag}, c.rws, so);
  CFENCE();
  STAMP(2);
}

constexpr int NXS = 4, NWS = 5, PRE = 5;
constexpr bool qIsX(int q) { return q % 3 == 0; }
constexpr int qIdx(int q) { return q % 3 == 0 ? q / 3 : 2 * (q / 3) + (q % 3) - 1; }
constexpr int qSlotOff(int q) { return qIsX(q) ? (qIdx(q) % NXS) * SLOTB : (NXS + qIdx(q) % NWS) * SLOTB; }
constexpr int qFree(int q) { return qIsX(q) ? (qIdx(q) < NXS ? 0 : 2 * (qIdx(q) - NXS) + 2) : (qIdx(q) < NWS ? 0 : qIdx(q) - NWS + 1); }
constexpr int needQ(int st) { return 3 * (st / 2) + 1 + (st & 1); }
constexpr int issuedBefore(int st) {
  int n = PRE;
  for (int s = 0; s < st; s++)
    for (int j = 0; j < 2; j++)
      if (n < 30 && qFree(n) <= s) n++;
  return n;
}
static_assert(issuedBefore(11) <= needQ(16), "the counter load is older than an entry that barrier 15 waits for");
template <int N> DI void WAITBARV() { asm volatile("s_waitcnt vmcnt(%0)\n\ts_barrier" ::"i"(N) : "memory"); }

constexpr int WDL = 20, WD0 = 16;
static_assert(issuedBefore(WD0) == 30, "no LDS-DMA is issued behind the register loads");
DI void glu_phase(Ctx& c, u32 raw, u32& epoch, v4u (&wd)[30]) {
  const int l = c.l, w = c.w, q = l >> 4, r = l & 15;
  const int panel = (c.xcd & 1) * 8 + (c.idx & 7), sub = c.idx >> 3, grp = (c.xcd >> 1) * 3 + sub;
#define ROT 10
  const int rot = ROT == 10 ? ((c.idx & 7) + 3 * sub) % 10 : (ROT == 2 ? 5 * ((c.idx + sub) & 1) : (ROT == 3 ? 3 * ((c.idx + sub) % 3) : 0));
  const rsrc_t rw = mkrs(w >= 4 ? c.wu : c.wg, 640 * 3072);
  u32 xvo[2];
  sfor<2>([&](auto I) { constexpr int i = I; xvo[i] = (u32)((panel * 128 + 8 * (2 * w + i) + (l >> 3)) * 1280 + (((l & 7) ^ ((4 * i + q) & 7)) * 16)); });
  const u32 wvo = (u32)((8 * (w & 3) + q) * 3072 + grp * 256 + ((r ^ (4 * q + 2 * (w & 1))) * 16));
  const u32 m0b = c.lb + w * 2048;
  auto dma = [&](auto Q, auto I) {
    constexpr int qq = Q, i = I;
    int blk = qq / 3 + rot;
    blk = blk >= 10 ? blk - 10 : blk;
    if constexpr (qIsX(qq)) DMA(m0b + qSlotOff(qq) + i * 1024, xvo[i], c.rx, (u32)(blk * 128));
    else DMA(m0b + qSlotOff(qq) + i * 1024, wvo, rw, (u32)((blk * 64 + (qIdx(qq) & 1) * 32 + i * 4) * 3072));
  };
  p1_third(c, raw, epoch, [&](auto Q) { sfor<2>([&](auto I) { dma(Q, I); }); });
  if (w >= 4) __builtin_amdgcn_s_setprio(1);
  const int sh = w & 1, ch = w >> 1;
  u32 ta[3][2], xa[2];
  sfor<6>([&](auto Z) {
    constexpr int z = Z / 2, t = Z & 1;
    ta[z][t] = c.lb + z * 65536 + (u32)((8 * q + (r >> 2)) * 256 + (((2 * ch + t) ^ (2 * (r >> 2) + (q & 1))) * 32) + (l & 3) * 8);
    OPAQUE(ta[z][t]);
  });
  sfor<2>([&](auto M) { constexpr int m = M; xa[m] = c.lb + (u32)((sh * 64 + r) * 128 + (((4 * m + q) ^ (r >> 1)) * 16)); OPAQUE(xa[m]); });
  f32x4 acc[4][4];
  bf16x8 wf[2][4], xf[2][4];
  auto rdw = [&](auto ST, auto T) {
    constexpr int st = ST, t = T, off = (NXS + st % NWS) * SLOTB + (t >> 1) * 8192;
    wf[st & 1][t] = cat(trr(ta[off >> 16][t & 1] + (off & 65535)), trr(ta[off >> 16][t & 1] + (off & 65535) + 1024));
  };
  auto rdx = [&](auto ST, auto I) {
    constexpr int st = ST, i = I, off = ((st >> 1) % NXS) * SLOTB + i * 2048;
    xf[st & 1][i] = *(LAS bf16x8*)(xa[st & 1] + off);
  };
  WAITBAR<2 * (PRE - 1 - needQ(1)) + 3>();
  STAMP(3);
  sfor<4>([&](auto I) { rdw(IC(0), I); rdx(IC(0), I); });
  GAP();
  v2u sg[4], cg[4];
  u32 fw;
  auto side_loads = [&]() {
    sfor<4>([&](auto I) {
      sg[I] = ld64<SC1>(c.rws, WS_STAT + (panel * 128 + sh * 64 + I * 16 + r) * 8);
      cg[I] = ld64<SC1>(c.rws, WS_CS + ((I >> 1) * 1536 + grp * 128 + ch * 32 + (I & 1) * 16 + r) * 8);
    });
  };
  sfor<20>([&](auto ST) {
    constexpr int st = ST;
    if constexpr (st > 0 && st < 19) {
      constexpr int n = 2 * (issuedBefore(st) - 1 - needQ(st + 1)) + (st > WD0 ? 5 * (st - WD0) : 0);
      static_assert(n >= 0, "");
      WAITBARV<n>();
    }
    if constexpr (st == 1) {
      static_assert(needQ(2) == PRE - 1, "");
      st32<SC1>(c.tag, c.rws, threadIdx.x == 0 ? WS_FLAG + (F_Z + 64 * sub + c.xcd * 8 + (c.idx & 7)) * 128 : OOB);
      GAP();
    }
    if constexpr (st == WD0 - 5) BLD32(fw, w == 0 ? WS_FLAG + (F_W + l) * 128 : OOB, c.rws);
    if constexpr (st == WD0 - 1) {
      OPAQUE(fw);
      if (w == 0 && !__all(fw == c.tag)) poll_flags(c, F_W + l);
      GAP();
    }
    if constexpr (st == 19) WAITBAR<63>();
    constexpr int q0 = issuedBefore(st), q1 = issuedBefore(st + 1);
    sfor<16>([&](auto G) {
      constexpr int g = G, t = g >> 2, i = g & 3;
      acc[t][i] = mfma16(wf[st & 1][t], xf[st & 1][i], st == 0 ? f32x4{0.f, 0.f, 0.f, 0.f} : acc[t][i]);
      if constexpr (st + 1 < 20) {
        if constexpr (i == 3) rdw(IC(st + 1), IC(t));
        if constexpr (i == 1) rdx(IC(st + 1), IC(t));
      }
      if constexpr (i == 2 && q0 + (g >> 3) < q1) dma(IC(q0 + (g >> 3)), IC((g >> 2) & 1));
      if constexpr (st >= WD0 && g % 3 == 2) wd_load<5 * (st - WD0) + g / 3>(c, wd);
      if constexpr (st == 18 && g == 0) side_loads();
      GAP();
    });
  });
  STAMP(4);
  auto side_ok = [&]() {
    bool ok = true;
    sfor<4>([&](auto I) { ok = ok && sg[I].y == c.tag && cg[I].y == c.tag; });
    return __all(ok);
  };
  if (!side_ok()) {
#pragma nounroll
    for (u32 n = 0; n < SPIN_CAP; ++n) {
      __builtin_amdgcn_s_sleep(16);
      side_loads();
      if (side_ok()) break;
    }
  }
  CFENCE();
  __builtin_amdgcn_s_setprio(0);
  bf16x8 cf[4], mf[4];
  float r2[4];
  sfor<4>([&](auto T) {
    const float s0 = q == 0 ? __uint_as_float(cg[T].x) : 0.f;
    const float a0 = rbf(s0), a1 = rbf(s0 - a0);
    cf[T] = asbf(v4u{cvtpk(a0, a1), cvtpk(s0 - a0 - a1, 0.f), 0u, 0u});
    const u32 nm = (sg[T].x & 0xffffu) ^ 0x8000u, nm2 = nm | (nm << 16);
    mf[T] = asbf(v4u{nm2, nm2, 0u, 0u});
    const float ri = __builtin_amdgcn_rcpf(bfhi(sg[T].x));
    r2[T] = ri * ri;
  });
  sfor<4>([&](auto I) {
    constexpr int i = I;
    sfor<4>([&](auto T) { acc[T][i] = mfma16(cf[T], mf[i], acc[T][i]); });
    v4u o;
    sfor<4>([&](auto P) {
      constexpr int t = P >> 1, e = 2 * (P & 1);
      o[(int)P] = cvtpk(acc[t][i][e] * acc[t + 2][i][e] * r2[i], acc[t][i][e + 1] * acc[t + 2][i][e + 1] * r2[i]);
    });
    st128<SC1>(o, c.rws, l * 16, WS_H + ((panel * 8 + sh * 4 + i) * 48 + grp * 4 + ch) * 1024);
  });
  STAMP(5);
  __builtin_amdgcn_s_barrier();
  CFENCE();
  sfor<30 - WDL>([&](auto I) { wd_load<WDL + I>(c, wd); });
  STAMP(6);
  arrive_publish<30 - WDL>(c, C_ARRA, F_H + 12 * panel + grp);
  STAMP(7);
}

constexpr u32 S_W = 0, S_WP = 656, S_BP = 784, S_CP = 1568, S_U = 50176, S_UP = 1352;
constexpr u32 S_FI = 94208, S_DI = S_FI + 8192, S_CS = S_DI + 4096, S_SCR = S_CS + 2048;
static_assert(128 * S_WP <= S_FI && 32 * S_CP <= S_U && S_U + 32 * S_UP <= S_FI, "");
template <int NS> DI void p3_groups(const D32& d, const v4u (&mf)[3][2], const u32 (&va)[3], const u32 (&ua)[3]) {
  const f32x4 z4 = {0.f, 0.f, 0.f, 0.f};
  bf16x8 vr[NS], vi[NS];
  sfor<NS>([&](auto S) { vr[S] = cat(trr(va[S]), trr(va[S] + 16 * S_CP)); vi[S] = cat(trr(va[S] + 8), trr(va[S] + 16 * S_CP + 8)); });
  sfor<NS>([&](auto S) {
    sfor<2>([&](auto I) {
      const bf16x8 y = pack44(mfma16(vi[S], d.n[I], mfma16(vr[S], d.c[I], z4)), mfma16(vr[S], d.s[I], mfma16(vi[S], d.c[I], z4)));
      sfor<2>([&](auto C) { *(LAS v2u*)(ua[S] + I * 16 * S_UP + C * 32) = pack4(mfma16(asbf(mf[S][C]), y, z4)); });
    });
  });
}
struct F64 { bf16x8 c[2][2], s[2][2], n[2][2]; };
template <int NU> DI void p2_units(const F64& f, const f32x4 (&wr)[2][2][2], const f32x4 (&wi)[2][2][2], u32 ra, u32 wa) {
  const f32x4 z4 = {0.f, 0.f, 0.f, 0.f};
  bf16x8 vr[NU][2], vi[NU][2], pr[NU][2], pi[NU][2];
  sfor<2 * NU>([&](auto J) {
    constexpr int S = J >> 1, P = J & 1;
    const u32 ad = ra + S * 576 + P * S_BP;
    vr[S][P] = cat(trr(ad), trr(ad + 32 * S_BP)); vi[S][P] = cat(trr(ad + 8), trr(ad + 32 * S_BP + 8));
  });
  CFENCE();
  sfor<2 * NU>([&](auto J) {
    constexpr int S = J >> 1, P = J & 1;
    const f32x4 er = mfma16(f.s[P][0], vi[S][0], mfma16(f.c[P][0], vr[S][0], z4)), ei = mfma16(f.n[P][0], vr[S][0], mfma16(f.c[P][0], vi[S][0], z4));
    const f32x4 od = mfma16(f.s[P][1], vi[S][1], mfma16(f.c[P][1], vr[S][1], z4)), oi = mfma16(f.n[P][1], vr[S][1], mfma16(f.c[P][1], vi[S][1], z4));
    const f32x4 ar = er + od, ai = ei + oi, br = er - od, bi = ei - oi;
    pr[S][P] = pack44(ar * wr[S][P][0] - ai * wi[S][P][0], br * wr[S][P][1] - bi * wi[S][P][1]);
    pi[S][P] = pack44(ar * wi[S][P][0] + ai * wr[S][P][0], br * wi[S][P][1] + bi * wr[S][P][1]);
  });
  sfor<2 * NU>([&](auto J) {
    constexpr int S = J >> 1, P = J & 1;
    const f32x4 er = mfma16(pi[S][0], f.n[P][0], mfma16(pr[S][0], f.c[P][0], z4)), ei = mfma16(pr[S][0], f.s[P][0], mfma16(pi[S][0], f.c[P][0], z4));
    const u32 ad = wa + S * 576 + P * S_BP;
    const f32x4 od = mfma16(pi[S][1], f.n[P][1], mfma16(pr[S][1], f.c[P][1], z4)), oi = mfma16(pr[S][1], f.s[P][1], mfma16(pi[S][1], f.c[P][1], z4));
    *(LAS bf16x8*)ad = pack44(er + od, ei + oi);
    *(LAS bf16x8*)(ad + 32 * S_BP) = pack44(er - od, ei - oi);
  });
}

DI void spc_phase(Ctx& c, u32 raw, u32& epoch, v4u (&wd)[30]) {
  const int l = c.l, w = c.w, q = l >> 4, r = l & 15, qd = c.xcd >> 1, rr = l >> 2;
  const int si = c.xcd * 8 + (c.idx & 7), hf = si & 1;
  const rsrc_t rt = mkrs(&TABLES, T_END);
  v4u fv[12], dv[4];
  const rsrc_t rf = mkrs(w < 4 ? c.wr : c.wi, 2048 * 642);
  const u32 fo = ((si >> 1) + 32 * (16 * (w & 3) + q)) * 642 + r * 16, fo2 = r < 8 ? fo + 512 : (r == 8 ? fo + 498 : OOB), fo3 = fo2 + (r == 8 ? 4 : 0);
  auto f_load = [&](auto P) {
    constexpr u32 o = P * 128 * 642 - (P >> 1) * 4;
    fv[3 * P] = ld128(rf, fo, o); fv[3 * P + 1] = ld128(rf, fo + 256, o); fv[3 * P + 2] = ld128(rf, P < 2 ? fo2 : fo3, o);
  };
  DMA(S_FI + w * 1024, l * 16, rt, T_F + w * 1024);
  const int it = si * 8 + w, cj = (c.xcd & 1) * 8 + (c.idx & 7);
  if (hf) { f_load(IC(2)); f_load(IC(3)); f_load(IC(0)); f_load(IC(1)); }
  else { f_load(IC(0)); f_load(IC(1)); f_load(IC(2)); f_load(IC(3)); }
  {
    const u32 vo = it < 480 ? (u32)(((it / 10) * 32 + (l >> 3)) * 1280 + (it % 10) * 128 + (l & 7) * 16) : OOB;
    sfor<4>([&](auto I) { dv[I] = ld128(mkrs(c.wd, 1536 * 1280), vo, I * 8 * 1280); });
  }
  CFENCE();
  tag_wait(c, raw, epoch);
  if (w < 4) d32_make(l, w, si, true, S_DI);
  sfor<12>([&](auto I) { if (I % 3 < 2 || r < 9) *(LAS v4u*)(S_W + (16 * w + 4 * (I / 3) + q) * S_WP + r * 16 + (I % 3) * 256) = fv[I]; });
  STAMP(3);
  WAITBAR<4>();
  STAMP(4);
  {
    v4u gd[4];
    tile_tr(c, dv, gd, S_SCR + w * 2048);
    sfor<4>([&](auto J) {
      const int c16 = (it % 10) * 4 + J;
      st128<SC1>(gd[J], c.rws, it < 480 ? WS_WD + (((c16 / 5) * 48 + it / 10) * 5 + c16 % 5) * 1024 + l * 16 : OOB);
    });
  }
  CFENCE();
  v4u cv[10];
  const rsrc_t rw = mkrs(cj >= 6 ? c.wu : c.wg, 640 * 3072);
  const u32 cvo = cj < 12 ? (u32)((8 * w + (l >> 3)) * 3072 + (384 * qd + 64 * (cj % 6)) * 2 + (l & 7) * 16) : OOB;
  sfor<5>([&](auto I) { cv[I] = ld128(rw, cvo, I * 64 * 3072); });
  CFENCE();
  const bool two = w < 3 - hf;
  f32x4 wr[2][2][2], wi[2][2][2];
  sfor<2>([&](auto S) {
    if (S == 0 || two) {
      const int kk = col_k(11 * hf + w + 8 * S, r);
      const float sc = ((kk == 0 || kk == 320) ? 1.0f : 2.0f) / (2048.0f * 640.0f);
      u32 ad = S_W + 8 * q * S_WP + 2 * kk + (kk >= 318 ? 14 : 4 * (q & 1)), ad2 = ad + 64 * S_WP;
      OPAQUE(ad2);
      sfor<16>([&](auto E) {
        constexpr int p = E >> 3, b = (E >> 2) & 1, e = E & 3, off = (2 * e + p + 32 * b) * S_WP;
        wr[S][p][b][e] = bflo(*(LAS u16*)(ad + off));
        wi[S][p][b][e] = bflo(*(LAS u16*)(ad2 + off));
      });
      sfor<4>([&](auto E) { wr[S][E >> 1][E & 1] *= sc; wi[S][E >> 1][E & 1] *= sc; PIN(wr[S][E >> 1][E & 1]); PIN(wi[S][E >> 1][E & 1]); });
    }
    if constexpr (S == 0) { CFENCE(); sfor<5>([&](auto I) { cv[5 + I] = ld128(rw, cvo, (5 + I) * 64 * 3072); }); CFENCE(); }
  });
  arrive_publish<10>(c, C_ARRW, F_W + si);
  F64 f;
  sfor<4>([&](auto P) {
    f.c[P >> 1][P & 1] = *(LAS bf16x8*)(S_FI + P * 1024 + l * 16);
    f.s[P >> 1][P & 1] = *(LAS bf16x8*)(S_FI + 4096 + P * 1024 + l * 16);
    f.n[P >> 1][P & 1] = neg8(f.s[P >> 1][P & 1]);
    PIN(f.n[P >> 1][P & 1]);
  });
  const int sl0 = w + (w >= (hf ? 3 : 7));
  u32 bb = S_W + (8 * w + q) * S_BP + r * 16, ra = S_W + sl0 * 64 + 2 * rr * S_BP + (l & 3) * 16, wa = S_W + sl0 * 64 + 2 * r * S_BP + q * 16;
  u32 zi = WS_Z + (si >> 1) * 98304 + hf * 768 + (8 * w + q) * 1536 + r * 16;
  u32 yo = WS_Y + (si >> 1) * 1536 + hf * 768 + (8 * w + q) * 49152 + r * 16;
  OPAQUE(bb); OPAQUE(ra); OPAQUE(wa); OPAQUE(zi); OPAQUE(yo);
  STAMP(5);
  {
    u32 k10 = 0x00003f80u, k01 = 0x3f800000u;
    asm volatile("" : "+s"(k10), "+s"(k01));
    float t[8] = {0.f, 0.f, 0.f, 0.f, 0.f, 0.f, 0.f, 0.f};
    sfor<40>([&](auto E) { constexpr int i = E / 4, e = E % 4; t[2 * e] = dot2(k10, cv[i][e], t[2 * e]); t[2 * e + 1] = dot2(k01, cv[i][e], t[2 * e + 1]); });
#pragma unroll
    for (int e = 0; e < 4; ++e) t[e] = fold32(t[e], t[e + 4]);
    t[0] = fold16(t[0], t[2]); t[1] = fold16(t[1], t[3]);
    const bool up = l & 8;
    *(LAS float*)(S_CS + w * 256 + (8 * (l & 7) + (l >> 3)) * 4) = (up ? t[1] : t[0]) + dpp<0x128>(up ? t[0] : t[1]);
  }
  STAMP(6);
  if (w >= 6) poll_flags(c, F_Z + 64 * (hf + w - 6) + l);
  WAITBAR<63>();
  STAMP(7);
  v4u sf[3][2], mf[3][2];
  {
    v4u y[6];
    sfor<6>([&](auto I) { y[I] = ld128<SC1>(c.rws, zi + (I % 3) * 256, (I / 3) * 4 * 1536); });
    GAP();
    float s = *(LAS float*)(S_CS + (l >> 3) * 256 + (w * 8 + (l & 7)) * 4);
    s += dpp<0x128>(s);
    s = fold16(s, s);
    s = fold32(s, s);
    st64<SC1>(v2u{__float_as_uint(s), c.tag}, c.rws, l < 8 && cj < 12 ? WS_CS + ((cj / 6) * 1536 + 384 * qd + 64 * (cj % 6) + w * 8 + l) * 8 : OOB);
    GAP();
    sfor<6>([&](auto I) { *(LAS v4u*)(bb + (I / 3) * 4 * S_BP + (I % 3) * 256) = y[I]; });
  }
  WAITBAR<63>();
  STAMP(8);
  if (two) p2_units<2>(f, wr, wi, ra, wa);
  else p2_units<1>(f, wr, wi, ra, wa);
  WAITBAR<63>();
  STAMP(9);
  sfor<6>([&](auto I) { st128<SC1>(*(LAS v4u*)(bb + (I / 3) * 4 * S_BP + (I % 3) * 256), c.rws, yo + (I % 3) * 256 + (I / 3) * 4 * 49152); });
  CFENCE();
  __builtin_amdgcn_s_barrier();
  sfor<6>([&](auto J) { mf[J >> 1][J & 1] = ld128(rt, (J < 4 || w < 5) ? l * 16 : OOB, (w + 8 * (J >> 1)) * 4096 + (2 + (J & 1)) * 1024); });
  WAITBAR<6>();
  if (w == 6) raise(c, F_Y + si);
  STAMP(10);
  sfor<6>([&](auto J) { sf[J >> 1][J & 1] = ld128(rt, l * 16, T_S + 6144 + J * 1024); });
  CFENCE();
  D32 d;
  d32_read(d, l, S_DI);
  PIN(d.n[0]); PIN(d.n[1]);
  u32 yi = WS_Y + si * 49152 + (4 * w + q) * 1536 + r * 16, cw = S_W + (4 * w + q) * S_CP + r * 16;
  u32 ur = S_U + 4 * w * S_UP + l * 8, ut = S_U + 4 * w * S_UP + 1024 + (l < 40 ? l : (l & 7) + 32) * 8;
  u32 ao = WS_A + (64 * (4 * w + (q & 1)) + si) * 1280 + r * 32 + (q >> 1) * 16, ao2 = r < 8 ? ao : OOB, ga[3], gb[3];
  sfor<3>([&](auto S) { const int g = w + 8 * S; ga[S] = S_W + rr * S_CP + (g + g / 7) * 64 + (l & 3) * 16; gb[S] = S_U + r * S_UP + g * 64 + q * 8; OPAQUE(ga[S]); OPAQUE(gb[S]); });
  OPAQUE(yi); OPAQUE(cw); OPAQUE(ur); OPAQUE(ut); OPAQUE(ao); OPAQUE(ao2);
  if (w == 7) poll_flags(c, F_Y + l);
  WAITBAR<63>();
  STAMP(11);
  {
    v4u y[6];
    sfor<6>([&](auto I) { y[I] = ld128<SC1>(c.rws, yi + I * 256); });
    GAP();
    __builtin_amdgcn_s_barrier();
    sfor<6>([&](auto I) { wd_load<I>(c, wd); });
    GAP();
    sfor<6>([&](auto I) { *(LAS v4u*)(cw + I * 256) = y[I]; });
  }
  WAITBAR<63>();
  STAMP(12);
  if (w < 5) p3_groups<3>(d, mf, ga, gb);
  else p3_groups<2>(d, mf, ga, gb);
  GAP();
  sfor<6>([&](auto I) { wd_load<6 + I>(c, wd); });
  WAITBAR<63>();
  STAMP(13);
  sfor<2>([&](auto Z) {
    f32x4 o[2][3];
    sfor<2>([&](auto Y) { stage40<3>(ur + (2 * Z + Y) * S_UP, ut + (2 * Z + Y) * S_UP, sf, o[Y]); });
    sfor<3>([&](auto T) {
      const v2u a = pack4(o[0][T]), b = pack4(o[1][T]);
      const auto x = __builtin_amdgcn_permlane16_swap(a.x, b.x, false, false), y = __builtin_amdgcn_permlane16_swap(a.y, b.y, false, false);
      st128<SC1>(v4u{x[0], y[0], x[1], y[1]}, c.rws, (T < 2 ? ao : ao2) + T * 512 + Z * 128 * 1280);
    });
  });
  WAITBAR<63>();
  STAMP(14);
  sfor<6>([&](auto I) { wd_load<12 + I>(c, wd); });
  arrive_publish<6>(c, C_ARRA, F_A + si);
  STAMP(15);
  sfor<12>([&](auto I) { wd_load<18 + I>(c, wd); });
  CFENCE();
}

constexpr u32 OUT_A = 0, OUT_B = 61440, OUT_S = 122880, OUT_O = OUT_S + 20480, OUT_OP = 176;
DI void out_phase(const Ctx& c, const v4u (&wd)[30], u32 epoch) {
  const int l = c.l, w = c.w, q = l >> 4, r = l & 15, a = w & 3;
  const int rt = 4 * c.xcd + (c.idx & 3), ct = c.idx >> 2;
  constexpr int HW = 2;
  u32 ho[4];
  sfor<4>([&](auto I) { ho[I] = WS_H + ((rt * 4 + ((a + I) & 3)) * 48 + 6 * out_kw(c)) * 1024; });
  v4u hq[HW][4];
  flag_wait(c, w == 0, C_GOH, F_H + 12 * (rt >> 1) + (l < 12 ? l : 0));
  STAMP(16);
  if (w < 4) __builtin_amdgcn_s_setprio(1);
  sfor<HW>([&](auto I) { sfor<4>([&](auto ST) { hq[I][ST] = ld128<SC1>(c.rws, l * 16, ho[ST] + I * 1024); }); });
  GAP();
  f32x4 acc[5][4];
  sfor<6>([&](auto I) {
    constexpr int i = I;
    sfor<4>([&](auto ST) {
      sfor<5>([&](auto T) { acc[T][ST] = mfma16(asbf(wd[5 * i + T]), asbf(hq[i % HW][ST]), i == 0 ? f32x4{0.f, 0.f, 0.f, 0.f} : acc[T][ST]); });
      if constexpr (i + HW < 6) hq[i % HW][ST] = ld128<SC1>(c.rws, l * 16, ho[ST] + (i + HW) * 1024);
      GAP();
    });
  });
  STAMP(17);
  __builtin_amdgcn_s_setprio(0);
  const u32 pb = (w < 4 ? OUT_A : OUT_B) + l * 16;
  sfor<3>([&](auto I) { sfor<5>([&](auto T) { *(LAS f32x4*)(pb + (((a + I + 1) & 3) * 3 + I) * 5120 + T * 1024) = acc[T][I + 1]; }); });
  if (w < 4) {
    if (w == 0) poll_flags(c, F_A + l);
    group_barrier(c, C_OUTA, 4);
    STAMP(18);
    v4u xs[3], as[3];
    u32 vo[3], lo[3];
    sfor<3>([&](auto I) {
      const u32 ch = 64 * I + l, row = (ch * 6554u) >> 16, cc = ch - 10 * row;
      vo[I] = ch < 160 ? (rt * 64 + a * 16 + row) * 1280 + ct * 160 + cc * 16 : OOB;
      lo[I] = OUT_O + (a * 16 + (row & 15)) * OUT_OP + cc * 16;
      xs[I] = ld128(c.rx, vo[I]); as[I] = ld128<SC1>(c.rws, vo[I], WS_A);
    });
    GAP();
    sfor<5>([&](auto T) {
      f32x4 s = acc[T][0];
      sfor<3>([&](auto J) { s += *(LAS f32x4*)(pb + (a * 3 + J) * 5120 + T * 1024); });
      *(LAS f32x4*)(OUT_S + (a * 5 + T) * 1024 + l * 16) = s;
    });
    STAMP(19);
    WAITBAR<63>();
    sfor<12>([&](auto E) { constexpr int i = E / 4, e = E % 4; xs[i][e] = cvtpk(bflo(xs[i][e]) + bflo(as[i][e]), bfhi(xs[i][e]) + bfhi(as[i][e])); });
    sfor<3>([&](auto I) { PIN(xs[I]); });
    WAITBAR<63>();
    STAMP(20);
    const rsrc_t ro = mkrs(c.out, 2048 * 1280);
    sfor<3>([&](auto I) {
      const v4u b = *(LAS v4u*)lo[I];
      v4u o;
      sfor<4>([&](auto E) { o[(int)E] = cvtpk(bflo(xs[I][(int)E]) + bflo(b[(int)E]), bfhi(xs[I][(int)E]) + bfhi(b[(int)E])); });
      st128<SC1>(o, ro, vo[I]);
    });
  } else {
    STAMP(19);
    WAITBAR<63>();
    sfor<5>([&](auto T) {
      f32x4 s = acc[T][0] + *(LAS f32x4*)(OUT_S + (a * 5 + T) * 1024 + l * 16);
      sfor<3>([&](auto J) { s += *(LAS f32x4*)(pb + (a * 3 + J) * 5120 + T * 1024); });
      *(LAS v2u*)(OUT_O + (a * 16 + r) * OUT_OP + T * 32 + q * 8) = pack4(s);
    });
    WAITBAR<63>();
    if (w == 4 && blockIdx.x == 0) {
      if (l == 0) st32<SC1>(epoch, c.rws, 0);
    }
  }
}

__global__ __launch_bounds__(512, 2) void k_all(const u16* x, const u16* wr, const u16* wi, const u16* wg, const u16* wu, const u16* wd, u16* out, char* ws) {
  Ctx c;
  c.ws = ws;
  c.rx = mkrs(x, 2048 * 1280);
  c.rws = mkrs(ws, WS_END);
  c.wr = wr; c.wi = wi; c.wg = wg; c.wu = wu; c.wd = wd; c.out = out;
  c.l = threadIdx.x & 63;
  c.w = __builtin_amdgcn_readfirstlane(threadIdx.x >> 6);
  c.xcd = blockIdx.x & 7;
  c.idx = blockIdx.x >> 3;
  u32 raw, epoch;
  asm volatile("s_load_dword %0, %1, 0x0" : "=s"(raw) : "s"(ws) : "memory");
  if (threadIdx.x < C_N) *(LAS u32*)(c.lb + L_CTL + threadIdx.x * 4) = 0;
  STAMP(0);
  const bool spc = (c.idx >> 3) == 3;
  v4u wdf[30];
  if (spc) spc_phase(c, raw, epoch, wdf);
  else glu_phase(c, raw, epoch, wdf);
  GAP();
  out_phase(c, wdf, epoch);
  STAMP(21);
  if (blockIdx.x == PROBE_WG && c.w == PROBE_W) {
    const u32 t0 = *(volatile LAS u32*)(L_CTL + 128 + c.w * 8), t1 = PROBE ? *(volatile LAS u32*)(L_CTL + 128 + c.w * 8 + 4) : t0;
    for (int i = 0; i < 100000 && (u32)__builtin_amdgcn_s_memrealtime() - t0 < 5000u + (t1 - t0); ++i) __builtin_amdgcn_s_sleep(1);
  }
}

extern "C" void kernel_launch(void* const* d_in, const int* in_sizes, int n_in, void* d_out, int out_size, void* d_ws, size_t ws_size, hipStream_t stream) {
  (void)hipFuncSetAttribute((const void*)k_all, hipFuncAttributeMaxDynamicSharedMemorySize, LDS_BYTES);
  k_all<<<256, 512, LDS_BYTES, stream>>>((const u16*)d_in[0], (const u16*)d_in[1], (const u16*)d_in[2], (const u16*)d_in[4], (const u16*)d_in[5],
                                         (const u16*)d_in[6], (u16*)d_out, (char*)d_ws);
}
```

```cpp
#include <hip/hip_runtime.h>
#include <utility>

typedef unsigned u32;
typedef unsigned short u16;
typedef unsigned long long u64;
typedef unsigned v4u __attribute__((ext_vector_type(4)));
typedef unsigned v2u __attribute__((ext_vector_type(2)));
typedef float f32x2 __attribute__((ext_vector_type(2)));
typedef float f32x4 __attribute__((ext_vector_type(4)));
typedef __bf16 bf16x2 __attribute__((ext_vector_type(2)));
typedef __bf16 bf16x4 __attribute__((ext_vector_type(4)));
typedef __bf16 bf16x8 __attribute__((ext_vector_type(8)));
typedef __amdgpu_buffer_rsrc_t rsrc_t;
#define LAS __attribute__((address_space(3)))
#define GAS __attribute__((address_space(1)))
#define DI __device__ __forceinline__
#define GAP() __builtin_amdgcn_sched_barrier(0)
#define IC(x) std::integral_constant<int, (x)>{}
#pragma clang diagnostic ignored "-Wint-to-pointer-cast"

template <class F, int... Is> DI void sfor_(F&& f, std::integer_sequence<int, Is...>) { (f(std::integral_constant<int, Is>{}), ...); }
template <int N, class F> DI void sfor(F&& f) { sfor_(f, std::make_integer_sequence<int, N>{}); }

constexpr u32 WS_STAT = 0x4000;
constexpr u32 WS_CS = 0x8000;
constexpr u32 WS_FLAG = 0x20000, F_Z = 0, F_H = 192, F_W = 384, F_Y = 448, F_A = 512;
constexpr u32 WS_Z = 0x40000;
constexpr u32 WS_Y = WS_Z + 0x300000;
constexpr u32 WS_WD = WS_Y + 0x300000;
constexpr u32 WS_H = WS_WD + 0x1E0000;
constexpr u32 WS_A = WS_H + 0x600000;
constexpr u32 WS_END = WS_A + 0x280000;
constexpr u32 OOB = 0x40000000u;
constexpr int SC1 = 16;
constexpr u32 SPIN_CAP = 1u << 16;
constexpr u32 LDS_BYTES = 163840, SLOTB = 16384;
constexpr u32 L_CTL = LDS_BYTES - 256;
enum { C_ARRZ = 0, C_ARRW = 1, C_ARRY = 3, C_ARRA = 4, C_GOZ = 5, C_GOY = 6, C_GOH = 7, C_GOA = 8, C_OUTA = 9, C_N = 16 };

DI u32 cvtpk(float lo, float hi) { f32x2 v = {lo, hi}; return __builtin_bit_cast(u32, __builtin_convertvector(v, bf16x2)); }
DI float bflo(u32 v) { return __uint_as_float(v << 16); }
DI float bfhi(u32 v) { return __uint_as_float(v & 0xffff0000u); }
DI float rbf(float f) { return bflo(cvtpk(f, f)); }
DI bf16x8 asbf(v4u v) { return __builtin_bit_cast(bf16x8, v); }
DI v4u asu(bf16x8 v) { return __builtin_bit_cast(v4u, v); }
DI rsrc_t mkrs(const void* p, u32 n) { return __builtin_amdgcn_make_buffer_rsrc((void*)p, 0, n, 0x00020000); }
DI void DMA(u32 m0v, u32 voff, rsrc_t rs, u32 soff) {
  asm volatile("s_mov_b32 m0, %0\n\ts_nop 0\n\tbuffer_load_dwordx4 %1, %2, %3 offen lds" ::"s"(m0v), "v"(voff), "s"(rs), "s"(soff) : "memory");
}
DI void BLD(v4u& d, u32 voff, rsrc_t rs, u32 soff) { asm volatile("buffer_load_dwordx4 %0, %1, %2, %3 offen" : "=v"(d) : "v"(voff), "s"(rs), "s"(soff) : "memory"); }
DI void BLD32(u32& d, u32 voff, rsrc_t rs) { asm volatile("buffer_load_dword %0, %1, %2, 0 offen sc1" : "=v"(d) : "v"(voff), "s"(rs) : "memory"); }
template <int N> DI void WAITBAR() { asm volatile("s_waitcnt vmcnt(%0) lgkmcnt(0)\n\ts_barrier" ::"i"(N) : "memory"); }
DI void DRAIN() { asm volatile("s_waitcnt vmcnt(0)" ::: "memory"); }
DI void OPAQUE(u32& v) { asm volatile("" : "+v"(v)); }
DI void PIN(bf16x8& v) { asm volatile("" : "+v"(v)); }
DI void PIN(v4u& v) { asm volatile("" : "+v"(v)); }
DI void PIN(f32x4& v) { asm volatile("" : "+v"(v)); }
DI void PIN(float& v) { asm volatile("" : "+v"(v)); }
DI void CFENCE() { asm volatile("" ::: "memory"); }
DI bf16x4 trr(u32 a) { return __builtin_amdgcn_ds_read_tr16_b64_v4bf16((LAS bf16x4*)a); }
DI bf16x8 cat(bf16x4 a, bf16x4 b) { return __builtin_shufflevector(a, b, 0, 1, 2, 3, 4, 5, 6, 7); }
DI f32x4 mfma16(bf16x8 a, bf16x8 b, f32x4 c) { return __builtin_amdgcn_mfma_f32_16x16x32_bf16(a, b, c, 0, 0, 0); }
template <int AUX = 0> DI v4u ld128(rsrc_t rs, u32 voff, u32 soff = 0) { return __builtin_amdgcn_raw_buffer_load_b128(rs, voff, soff, AUX); }
template <int AUX = 0> DI v2u ld64(rsrc_t rs, u32 voff, u32 soff = 0) { return __builtin_amdgcn_raw_buffer_load_b64(rs, voff, soff, AUX); }
template <int AUX = 0> DI u32 ld32(rsrc_t rs, u32 voff, u32 soff = 0) { return __builtin_amdgcn_raw_buffer_load_b32(rs, voff, soff, AUX); }
template <int AUX = 0> DI void st128(v4u v, rsrc_t rs, u32 voff, u32 soff = 0) { __builtin_amdgcn_raw_buffer_store_b128(v, rs, voff + soff, 0, AUX); }
template <int AUX = 0> DI void st64(v2u v, rsrc_t rs, u32 voff, u32 soff = 0) { __builtin_amdgcn_raw_buffer_store_b64(v, rs, voff, soff, AUX); }
template <int AUX = 0> DI void st32(u32 v, rsrc_t rs, u32 voff, u32 soff = 0) { __builtin_amdgcn_raw_buffer_store_b32(v, rs, voff, soff, AUX); }
DI float cosr(float rev) { return __builtin_amdgcn_cosf(rev); }
DI float sinr(float rev) { return __builtin_amdgcn_sinf(rev); }
DI float rev640(int p) {
  const float pf = (float)p;
  return __builtin_fmaf(__builtin_floorf(pf * (1.0f / 640.0f)), -640.0f, pf) * (1.0f / 640.0f);
}
template <int CTRL> DI float dpp(float v) { return __int_as_float(__builtin_amdgcn_update_dpp(0, __float_as_int(v), CTRL, 0xf, 0xf, true)); }
DI float fold32(float a, float b) {
  const auto r = __builtin_amdgcn_permlane32_swap(__float_as_uint(a), __float_as_uint(b), false, false);
  return __uint_as_float(r[0]) + __uint_as_float(r[1]);
}
DI float fold16(float a, float b) {
  const auto r = __builtin_amdgcn_permlane16_swap(__float_as_uint(a), __float_as_uint(b), false, false);
  return __uint_as_float(r[0]) + __uint_as_float(r[1]);
}
DI float wave_sum(float v) {
  v += dpp<0xB1>(v); v += dpp<0x4E>(v); v += dpp<0x141>(v); v += dpp<0x140>(v);
  v = fold16(v, v);
  return fold32(v, v);
}

#define STAMP(i)
struct Ctx {
  char* ws;
  rsrc_t rx, rws;
  const u16 *wr, *wi, *wg, *wu, *wd;
  u16* out;
  static constexpr u32 lb = 0;
  u32 tag;
  int l, w, xcd, idx;
};

DI u32 lds_ld(u32 a) { return __hip_atomic_load((LAS u32*)a, __ATOMIC_RELAXED, __HIP_MEMORY_SCOPE_WORKGROUP); }
DI void group_barrier(const Ctx& c, int word, u32 target) {
  asm volatile("s_waitcnt lgkmcnt(0)" ::: "memory");
  if (c.l == 0) __hip_atomic_fetch_add((LAS u32*)(c.lb + L_CTL + word * 4), 1u, __ATOMIC_RELAXED, __HIP_MEMORY_SCOPE_WORKGROUP);
#pragma nounroll
  for (u32 n = 0; n < (SPIN_CAP << 8) && lds_ld(c.lb + L_CTL + word * 4) < target; ++n) __builtin_amdgcn_s_sleep(1);
  CFENCE();
}
DI void raise(const Ctx& c, u32 flag) { if (c.l == 0) st32<SC1>(c.tag, c.rws, WS_FLAG + flag * 128); }
template <int N = 0> DI void arrive_publish(const Ctx& c, int word, u32 flag) {
  asm volatile("s_waitcnt vmcnt(%0)" ::"i"(N) : "memory");
  u32 old = 0;
  if (c.l == 0) old = __hip_atomic_fetch_add((LAS u32*)(L_CTL + word * 4), 1u, __ATOMIC_RELAXED, __HIP_MEMORY_SCOPE_WORKGROUP);
  old = __builtin_amdgcn_readfirstlane(old);
  if (old == 7u) raise(c, flag);
}
DI void poll_flags(const Ctx& c, u32 flag) {
#pragma nounroll
  for (u32 i = 0; i < SPIN_CAP && ld32<SC1>(c.rws, WS_FLAG + flag * 128) != c.tag; ++i) __builtin_amdgcn_s_sleep(2);
  CFENCE();
}
DI void flag_wait(const Ctx& c, bool leader, int word, u32 flag) {
  if (leader) {
    poll_flags(c, flag);
    if (c.l == 0) __hip_atomic_store((LAS u32*)(L_CTL + word * 4), 1u, __ATOMIC_RELAXED, __HIP_MEMORY_SCOPE_WORKGROUP);
  } else {
#pragma nounroll
    for (u32 i = 0; i < (SPIN_CAP << 6) && lds_ld(L_CTL + word * 4) == 0u; ++i) __builtin_amdgcn_s_sleep(1);
    CFENCE();
  }
}
DI void tag_wait(Ctx& c, u32 raw, u32& epoch) {
  asm volatile("s_waitcnt lgkmcnt(0)\n\ts_add_u32 %0, %1, 1" : "=s"(epoch) : "s"(raw) : "scc");
  c.tag = (epoch * 0x9E3779B1u) ^ 0x5bd1e995u;
}
DI int out_kw(const Ctx& c) { return (c.w + (c.idx >> 2) + 2 * (c.idx & 3)) & 7; }
template <int I> DI void wd_load(const Ctx& c, v4u (&wd)[30]) {
  wd[I] = ld128<SC1>(c.rws, c.l * 16 + (I % 4) * 1024, WS_WD + ((c.idx >> 2) * 48 + 6 * out_kw(c)) * 5120 + (I / 4) * 4096);
}

DI bf16x8 pack8(const float (&v)[8]) { return asbf(v4u{cvtpk(v[0], v[1]), cvtpk(v[2], v[3]), cvtpk(v[4], v[5]), cvtpk(v[6], v[7])}); }
DI bf16x8 pack44(f32x4 a, f32x4 b) { return asbf(v4u{cvtpk(a[0], a[1]), cvtpk(a[2], a[3]), cvtpk(b[0], b[1]), cvtpk(b[2], b[3])}); }
DI v2u pack4(f32x4 a) { return v2u{cvtpk(a[0], a[1]), cvtpk(a[2], a[3])}; }
DI bf16x8 neg8(bf16x8 v) { return asbf(asu(v) ^ 0x80008000u); }
DI float dot2(u32 a, u32 b, float c) { return __builtin_amdgcn_fdot2_f32_bf16(__builtin_bit_cast(bf16x2, a), __builtin_bit_cast(bf16x2, b), c, false); }
DI bf16x8 cos8(float a0, float da) {
  float cc[8];
  sfor<8>([&](auto E) { cc[E] = cosr(__builtin_fmaf(da, (float)E, a0)); });
  return pack8(cc);
}
DI void tile_tr(const Ctx& c, const v4u (&v)[4], v4u (&f)[4], u32 buf) {
  const int l = c.l, rr = 4 * (l >> 4) + ((l & 15) >> 2);
  const u32 wa = buf + (l >> 3) * 128 + (((l & 7) ^ (((l >> 4) & 3) << 1)) * 16), ra = buf + rr * 128 + (l & 3) * 8;
  bf16x4 t[2][4];
#pragma unroll
  for (int u = 0; u < 2; ++u) {
    *(LAS v4u*)wa = v[2 * u];
    *(LAS v4u*)(wa + 1024) = v[2 * u + 1];
    CFENCE();
#pragma unroll
    for (int j = 0; j < 4; ++j) t[u][j] = trr(ra + ((j ^ ((rr >> 1) & 3)) * 32));
    CFENCE();
  }
#pragma unroll
  for (int j = 0; j < 4; ++j) f[j] = asu(cat(t[0][j], t[1][j]));
}
struct Cos640 { u16 v[640]; };
constexpr Cos640 make_cos() {
  Cos640 t{};
  for (int i = 0; i < 640; ++i) {
    int n = i > 320 ? 640 - i : i;
    const bool neg = n > 160;
    if (neg) n = 320 - n;
    const double x = 6.283185307179586476925 * n / 640.0, x2 = x * x;
    double term = 1.0, sum = 1.0;
    for (int j = 1; j <= 12; ++j) { term *= -x2 / ((2 * j - 1) * (2 * j)); sum += term; }
    const float f = n == 160 ? 0.f : (float)(neg ? -sum : sum);
    const u32 u = __builtin_bit_cast(u32, f);
    t.v[i] = (u16)((u + 0x7fffu + ((u >> 16) & 1u)) >> 16);
  }
  return t;
}
constexpr Cos640 COS = make_cos();
constexpr int col_kc(int g, int m) { return g == 20 ? 320 : (g == 0 ? (m < 8 ? 40 * m : 40 * m - 300) : (g + 40 * m > 320 ? 640 - g - 40 * m : g + 40 * m)); }
constexpr u16 m_at(int g, int cc, int m, int cp, int d2) {
  const bool edge = g == 0 || g == 20, on = !edge || (g == 0 ? (m >> 3) == cp : (m == 0 && cp == 0));
  return on ? COS.v[(col_kc(g, m) * d2 + 160 * cc + ((cp && !edge) ? (g + 40 * m > 320 ? 160 : 480) : 0)) % 640] : (u16)0;
}
struct MGroup { u16 v[4][512]; };
constexpr MGroup make_m(int g) {
  MGroup t{};
  for (int h = 0; h < 2; ++h)
    for (int l = 0; l < 64; ++l)
      for (int e = 0; e < 8; ++e) {
        t.v[h][l * 8 + e] = m_at(g, h, l & 15, l >> 5, 8 * ((l >> 4) & 1) + e);
        t.v[2 + h][l * 8 + e] = m_at(g, e >> 2, 4 * (l >> 4) + (e & 3), h, l & 15);
      }
  return t;
}
template <int G> constexpr MGroup M_OF = make_m(G);
struct Small { u16 s[6][1024]; u16 f[8][512]; };
constexpr Small make_small() {
  Small t{};
  for (int w = 0; w < 6; ++w)
    for (int l = 0; l < 64; ++l)
      for (int e = 0; e < 12; ++e) {
        const int k = 16 * (e >> 2) + 4 * (l >> 4) + (e & 3), j = w < 3 ? 14 * w + (l & 15) : k, d1 = w < 3 ? k : 16 * (w - 3) + (l & 15);
        const int g = j == 1 ? 20 : (j == 40 ? 0 : j >> 1);
        t.s[w][(e >> 3) * 512 + l * 8 + (e & 7)] = (j <= 40 && d1 < 40) ? COS.v[(16 * g * d1 + (((j & 1) && j != 1) ? 160 : 0)) % 640] : (u16)0;
      }
  for (int w = 0; w < 8; ++w)
    for (int l = 0; l < 64; ++l)
      for (int e = 0; e < 8; ++e)
        t.f[w][l * 8 + e] = COS.v[(10 * (2 * (l & 15) + ((w >> 1) & 1)) * (2 * (4 * (l >> 4) + (e & 3)) + (w & 1) + 32 * (e >> 2)) + (w >= 4 ? 480 : 0)) % 640];
  return t;
}
constexpr Small SMALL = make_small();
struct Tables { MGroup m[21]; Small s; };
template <int... G> constexpr Tables make_tables(std::integer_sequence<int, G...>) { return Tables{{M_OF<G>...}, SMALL}; }
static __device__ __attribute__((aligned(128))) const Tables TABLES = make_tables(std::make_integer_sequence<int, 21>{});
constexpr u32 T_S = 21 * 4096, T_F = T_S + 12288, T_END = T_F + 8192;
static_assert(sizeof(Tables) == T_END, "");

DI int col_k(int g, int m) {
  const int kp = g + 40 * m;
  return g == 20 ? 320 : (g == 0 ? (m < 8 ? kp : kp - 300) : (kp > 320 ? 640 - kp : kp));
}
template <int NT> DI void stage40(u32 a, u32 tail, const v4u (&f)[NT][2], f32x4 (&acc)[NT]) {
  const bf16x8 x0 = cat(trr(a), trr(a + 512)), x1 = cat(trr(tail), trr(tail));
  sfor<NT>([&](auto T) { acc[T] = mfma16(x1, asbf(f[T][1]), mfma16(x0, asbf(f[T][0]), f32x4{0.f, 0.f, 0.f, 0.f})); });
}
struct D32 { bf16x8 c[2], s[2], n[2]; };
DI void d32_make(int l, int i, int sl, bool back, u32 base) {
  const int n = 16 * (i >> 1) + (l & 15);
  const float da = (float)(64 * n + (back ? sl : 0)) * (1.0f / 2048.0f);
  const float a0 = __builtin_fmaf(da, (float)(4 * (l >> 4)), (float)(back ? 0 : n * sl) * (1.0f / 2048.0f) - ((i & 1) ? 0.25f : 0.f));
  float cc[8];
  sfor<8>([&](auto E) { cc[E] = cosr(__builtin_fmaf(da, (float)(16 * (E >> 2) + (E & 3)), a0)); });
  *(LAS bf16x8*)(base + i * 1024 + l * 16) = pack8(cc);
}
DI void d32_read(D32& d, int l, u32 base) {
  sfor<2>([&](auto U) {
    d.c[U] = *(LAS bf16x8*)(base + 2 * U * 1024 + l * 16); d.s[U] = *(LAS bf16x8*)(base + (2 * U + 1) * 1024 + l * 16);
    d.n[U] = neg8(d.s[U]);
  });
}

constexpr u32 P_T = 32768, P_TP = 2064, P_V = 49280, P_VP = 464, P_D = 155648, P_X = 114688;
static_assert(P_T + 8 * P_TP <= P_V && P_V + 32 * P_VP + 64 <= 65536, "");
template <class F> DI void p1_third(Ctx& c, u32 raw, u32& epoch, F&& issue) {
  const int l = c.l, w = c.w, q = l >> 4, r = l & 15, sl = c.xcd * 8 + (c.idx & 7), c3 = c.idx >> 3;
  const rsrc_t rt = mkrs(&TABLES, T_END);
  v4u xv[5], sf[1][2], mf[2];
  const u32 xo = (64 * (4 * w + q) + sl) * 1280 + r * 16;
  sfor<5>([&](auto I) { BLD(xv[I], xo, c.rx, I * 256); });
  sfor<2>([&](auto I) { BLD(sf[0][I], l * 16, rt, T_S + c3 * 2048 + I * 1024); });
  sfor<2>([&](auto I) { BLD(mf[I], w < 7 ? l * 16 : OOB, rt, (7 * c3 + w) * 4096 + I * 1024); });
  __builtin_amdgcn_s_barrier();
  issue(IC(0));
  tag_wait(c, raw, epoch);
  if (w < 4) d32_make(l, w, sl, false, P_D);
  u32 xw = P_X + w * 5120 + q * 1280 + r * 16, xr = P_X + w * 5120 + l * 8, xt = P_X + w * 5120 + 1024 + (l & 31) * 8;
  u32 tw = P_T + (r >> 1) * P_TP + (r & 1) * 512 + (w >> 2) * 1024 + (q >> 1) * 256 + (4 * (w & 3)) * 16 + (q & 1) * 8;
  u32 ta = P_T + w * P_TP + l * 16, va = P_V + r * P_VP + w * 64 + q * 16, v0 = P_V + (4 * w + q) * P_VP + r * 16;
  u32 z0 = WS_Z + sl * 1536 + c3 * 512 + (4 * w + q) * 98304 + r * 16;
  u32 so = (r == 0 && c3 == 0) ? WS_STAT + (64 * (4 * w + q) + sl) * 8 : OOB, k11 = 0x3f803f80u;
  float dc = (c3 == 0 ? r == 0 : (c3 == 2 && r == 12)) ? -40.0f : 0.f;
  OPAQUE(xw); OPAQUE(xr); OPAQUE(xt); OPAQUE(tw); OPAQUE(ta); OPAQUE(va); OPAQUE(v0); OPAQUE(z0); OPAQUE(so); PIN(dc);
  asm volatile("" : "+s"(k11));
  asm volatile("s_waitcnt vmcnt(4)" : "+v"(xv[0]), "+v"(xv[1]), "+v"(xv[2]), "+v"(xv[3]), "+v"(xv[4]), "+v"(sf[0][0]), "+v"(sf[0][1])::"memory");
  STAMP(1);
  sfor<5>([&](auto I) { *(LAS v4u*)(xw + I * 256) = xv[I]; });
  issue(IC(1));
  float s0 = 0.f, s1 = 0.f, q0 = 0.f, q1 = 0.f;
  sfor<10>([&](auto E) {
    const u32 a = xv[E / 2][2 * (E & 1)], b = xv[E / 2][2 * (E & 1) + 1];
    s0 = dot2(k11, a, s0); s1 = dot2(k11, b, s1); q0 = dot2(a, a, q0); q1 = dot2(b, b, q1);
  });
  s0 += s1; q0 += q1;
  GAP();
  issue(IC(2));
  f32x4 acc[4][1];
  sfor<4>([&](auto Z) { stage40<1>(xr + Z * 1280, xt + Z * 1280, sf, acc[Z]); });
  s0 += dpp<0xB1>(s0); q0 += dpp<0xB1>(q0); s0 += dpp<0x4E>(s0); q0 += dpp<0x4E>(q0);
  s0 += dpp<0x141>(s0); q0 += dpp<0x141>(q0); s0 += dpp<0x140>(s0); q0 += dpp<0x140>(q0);
  const float mean = s0 * (1.0f / 640.0f), var = __builtin_fmaxf(__builtin_fmaf(q0, 1.0f / 640.0f, -mean * mean), 0.f);
  const u32 stat = cvtpk(mean, __builtin_amdgcn_sqrtf(rbf(rbf(var) + 1e-5f)));
  const float rinv = __builtin_amdgcn_rcpf(bfhi(stat)), mr = bflo(stat) * rinv;
  sfor<4>([&](auto Z) {
    const float ri = __uint_as_float(__builtin_amdgcn_readlane(__float_as_uint(rinv), 16 * Z));
    const float mz = __uint_as_float(__builtin_amdgcn_readlane(__float_as_uint(mr), 16 * Z)) * dc;
    *(LAS v2u*)(tw + Z * 16) = pack4(acc[Z][0] * ri + mz);
  });
  issue(IC(3));
  asm volatile("s_waitcnt vmcnt(8) lgkmcnt(0)\n\ts_barrier" : "+v"(mf[0]), "+v"(mf[1])::"memory");
  if (w < 7) {
    const f32x4 z4 = {0.f, 0.f, 0.f, 0.f};
    D32 d;
    d32_read(d, l, P_D);
    const bf16x8 t0 = *(LAS bf16x8*)ta, t1 = *(LAS bf16x8*)(ta + 1024);
    const bf16x8 zr = pack44(mfma16(t0, asbf(mf[0]), z4), mfma16(t1, asbf(mf[0]), z4)), zi = pack44(mfma16(t0, asbf(mf[1]), z4), mfma16(t1, asbf(mf[1]), z4));
    sfor<2>([&](auto U) {
      *(LAS bf16x8*)(va + U * 16 * P_VP) = pack44(mfma16(zi, d.s[U], mfma16(zr, d.c[U], z4)), mfma16(zr, d.n[U], mfma16(zi, d.c[U], z4)));
    });
  }
  issue(IC(4));
  WAITBAR<63>();
  st128<SC1>(*(LAS v4u*)v0, c.rws, z0);
  st128<SC1>(*(LAS v4u*)(v0 + 256), c.rws, z0 + 256);
  st64<SC1>(v2u{stat, c.tag}, c.rws, so);
  CFENCE();
  STAMP(2);
}

constexpr int NXS = 4, NWS = 5, PRE = 5;
constexpr bool qIsX(int q) { return q % 3 == 0; }
constexpr int qIdx(int q) { return q % 3 == 0 ? q / 3 : 2 * (q / 3) + (q % 3) - 1; }
constexpr int qSlotOff(int q) { return qIsX(q) ? (qIdx(q) % NXS) * SLOTB : (NXS + qIdx(q) % NWS) * SLOTB; }
constexpr int qFree(int q) { return qIsX(q) ? (qIdx(q) < NXS ? 0 : 2 * (qIdx(q) - NXS) + 2) : (qIdx(q) < NWS ? 0 : qIdx(q) - NWS + 1); }
constexpr int needQ(int st) { return 3 * (st / 2) + 1 + (st & 1); }
constexpr int issuedBefore(int st) {
  int n = PRE;
  for (int s = 0; s < st; s++)
    for (int j = 0; j < 2; j++)
      if (n < 30 && qFree(n) <= s) n++;
  return n;
}
static_assert(issuedBefore(11) <= needQ(16), "the counter load is older than an entry that barrier 15 waits for");
template <int N> DI void WAITBARV() { asm volatile("s_waitcnt vmcnt(%0)\n\ts_barrier" ::"i"(N) : "memory"); }

constexpr int WDL = 20, WD0 = 16;
static_assert(issuedBefore(WD0) == 30, "no LDS-DMA is issued behind the register loads");
DI void glu_phase(Ctx& c, u32 raw, u32& epoch, v4u (&wd)[30]) {
  const int l = c.l, w = c.w, q = l >> 4, r = l & 15;
  const int panel = (c.xcd & 1) * 8 + (c.idx & 7), sub = c.idx >> 3, grp = (c.xcd >> 1) * 3 + sub;
#define ROT 0
  const int rot = ROT == 10 ? ((c.idx & 7) + 3 * sub) % 10 : (ROT == 2 ? 5 * ((c.idx + sub) & 1) : (ROT == 3 ? 3 * ((c.idx + sub) % 3) : 0));
  const rsrc_t rw = mkrs(w >= 4 ? c.wu : c.wg, 640 * 3072);
  u32 xvo[2];
  sfor<2>([&](auto I) { constexpr int i = I; xvo[i] = (u32)((panel * 128 + 8 * (2 * w + i) + (l >> 3)) * 1280 + (((l & 7) ^ ((4 * i + q) & 7)) * 16)); });
  const u32 wvo = (u32)((8 * (w & 3) + q) * 3072 + grp * 256 + ((r ^ (4 * q + 2 * (w & 1))) * 16));
  const u32 m0b = c.lb + w * 2048;
  auto dma = [&](auto Q, auto I) {
    constexpr int qq = Q, i = I;
    int blk = qq / 3 + rot;
    blk = blk >= 10 ? blk - 10 : blk;
    if constexpr (qIsX(qq)) DMA(m0b + qSlotOff(qq) + i * 1024, xvo[i], c.rx, (u32)(blk * 128));
    else DMA(m0b + qSlotOff(qq) + i * 1024, wvo, rw, (u32)((blk * 64 + (qIdx(qq) & 1) * 32 + i * 4) * 3072));
  };
  p1_third(c, raw, epoch, [&](auto Q) { sfor<2>([&](auto I) { dma(Q, I); }); });
  if (w >= 4) __builtin_amdgcn_s_setprio(1);
  const int sh = w & 1, ch = w >> 1;
  u32 ta[3][2], xa[2];
  sfor<6>([&](auto Z) {
    constexpr int z = Z / 2, t = Z & 1;
    ta[z][t] = c.lb + z * 65536 + (u32)((8 * q + (r >> 2)) * 256 + (((2 * ch + t) ^ (2 * (r >> 2) + (q & 1))) * 32) + (l & 3) * 8);
    OPAQUE(ta[z][t]);
  });
  sfor<2>([&](auto M) { constexpr int m = M; xa[m] = c.lb + (u32)((sh * 64 + r) * 128 + (((4 * m + q) ^ (r >> 1)) * 16)); OPAQUE(xa[m]); });
  f32x4 acc[4][4];
  bf16x8 wf[2][4], xf[2][4];
  auto rdw = [&](auto ST, auto T) {
    constexpr int st = ST, t = T, off = (NXS + st % NWS) * SLOTB + (t >> 1) * 8192;
    wf[st & 1][t] = cat(trr(ta[off >> 16][t & 1] + (off & 65535)), trr(ta[off >> 16][t & 1] + (off & 65535) + 1024));
  };
  auto rdx = [&](auto ST, auto I) {
    constexpr int st = ST, i = I, off = ((st >> 1) % NXS) * SLOTB + i * 2048;
    xf[st & 1][i] = *(LAS bf16x8*)(xa[st & 1] + off);
  };
  WAITBAR<2 * (PRE - 1 - needQ(1)) + 3>();
  STAMP(3);
  sfor<4>([&](auto I) { rdw(IC(0), I); rdx(IC(0), I); });
  GAP();
  v2u sg[4], cg[4];
  u32 fw;
  auto side_loads = [&]() {
    sfor<4>([&](auto I) {
      sg[I] = ld64<SC1>(c.rws, WS_STAT + (panel * 128 + sh * 64 + I * 16 + r) * 8);
      cg[I] = ld64<SC1>(c.rws, WS_CS + ((I >> 1) * 1536 + grp * 128 + ch * 32 + (I & 1) * 16 + r) * 8);
    });
  };
  sfor<20>([&](auto ST) {
    constexpr int st = ST;
    if constexpr (st > 0 && st < 19) {
      constexpr int n = 2 * (issuedBefore(st) - 1 - needQ(st + 1)) + (st > WD0 ? 5 * (st - WD0) : 0);
      static_assert(n >= 0, "");
      WAITBARV<n>();
    }
    if constexpr (st == 1) {
      static_assert(needQ(2) == PRE - 1, "");
      st32<SC1>(c.tag, c.rws, threadIdx.x == 0 ? WS_FLAG + (F_Z + 64 * sub + c.xcd * 8 + (c.idx & 7)) * 128 : OOB);
      GAP();
    }
    if constexpr (st == 5 || st == 10 || st == 15) STAMP(7 + st / 5);
    if constexpr (st == 16) STAMP(11);
    if constexpr (st == WD0 - 5) BLD32(fw, w == 0 ? WS_FLAG + (F_W + l) * 128 : OOB, c.rws);
    if constexpr (st == WD0 - 1) {
      OPAQUE(fw);
      if (w == 0 && !__all(fw == c.tag)) poll_flags(c, F_W + l);
      GAP();
    }
    if constexpr (st == 19) WAITBAR<63>();
    constexpr int q0 = issuedBefore(st), q1 = issuedBefore(st + 1);
    sfor<16>([&](auto G) {
      constexpr int g = G, t = g >> 2, i = g & 3;
      acc[t][i] = mfma16(wf[st & 1][t], xf[st & 1][i], st == 0 ? f32x4{0.f, 0.f, 0.f, 0.f} : acc[t][i]);
      if constexpr (st + 1 < 20) {
        if constexpr (i == 3) rdw(IC(st + 1), IC(t));
        if constexpr (i == 1) rdx(IC(st + 1), IC(t));
      }
      if constexpr (i == 2 && q0 + (g >> 3) < q1) dma(IC(q0 + (g >> 3)), IC((g >> 2) & 1));
      if constexpr (st >= WD0 && g % 3 == 2) wd_load<5 * (st - WD0) + g / 3>(c, wd);
      if constexpr (st == 18 && g == 0) side_loads();
      GAP();
    });
  });
  STAMP(4);
  auto side_ok = [&]() {
    bool ok = true;
    sfor<4>([&](auto I) { ok = ok && sg[I].y == c.tag && cg[I].y == c.tag; });
    return __all(ok);
  };
  if (!side_ok()) {
#pragma nounroll
    for (u32 n = 0; n < SPIN_CAP; ++n) {
      __builtin_amdgcn_s_sleep(16);
      side_loads();
      if (side_ok()) break;
    }
  }
  CFENCE();
  __builtin_amdgcn_s_setprio(0);
  bf16x8 cf[4], mf[4];
  float r2[4];
  sfor<4>([&](auto T) {
    const float s0 = q == 0 ? __uint_as_float(cg[T].x) : 0.f;
    const float a0 = rbf(s0), a1 = rbf(s0 - a0);
    cf[T] = asbf(v4u{cvtpk(a0, a1), cvtpk(s0 - a0 - a1, 0.f), 0u, 0u});
    const u32 nm = (sg[T].x & 0xffffu) ^ 0x8000u, nm2 = nm | (nm << 16);
    mf[T] = asbf(v4u{nm2, nm2, 0u, 0u});
    const float ri = __builtin_amdgcn_rcpf(bfhi(sg[T].x));
    r2[T] = ri * ri;
  });
  sfor<4>([&](auto I) {
    constexpr int i = I;
    sfor<4>([&](auto T) { acc[T][i] = mfma16(cf[T], mf[i], acc[T][i]); });
    v4u o;
    sfor<4>([&](auto P) {
      constexpr int t = P >> 1, e = 2 * (P & 1);
      o[(int)P] = cvtpk(acc[t][i][e] * acc[t + 2][i][e] * r2[i], acc[t][i][e + 1] * acc[t + 2][i][e + 1] * r2[i]);
    });
    st128<SC1>(o, c.rws, l * 16, WS_H + ((panel * 8 + sh * 4 + i) * 48 + grp * 4 + ch) * 1024);
  });
  STAMP(5);
  arrive_publish(c, C_ARRA, F_H + 12 * panel + grp);
  STAMP(7);
}

constexpr u32 S_W = 0, S_WQ = 3088, S_BP = 784, S_CP = 1568, S_U = 50176, S_UP = 1352;
constexpr u32 S_FI = 99328, S_DI = S_FI + 8192, S_CS = S_DI + 4096, S_SCR = S_CS + 2048;
static_assert(32 * S_WQ <= S_FI && 32 * S_CP <= S_U && S_U + 32 * S_UP <= S_FI, "");
template <int NS> DI void p3_groups(const D32& d, const v4u (&mf)[3][2], const u32 (&va)[3], const u32 (&ua)[3]) {
  const f32x4 z4 = {0.f, 0.f, 0.f, 0.f};
  bf16x8 vr[NS], vi[NS];
  sfor<NS>([&](auto S) { vr[S] = cat(trr(va[S]), trr(va[S] + 16 * S_CP)); vi[S] = cat(trr(va[S] + 8), trr(va[S] + 16 * S_CP + 8)); });
  sfor<NS>([&](auto S) {
    sfor<2>([&](auto I) {
      const bf16x8 y = pack44(mfma16(vi[S], d.n[I], mfma16(vr[S], d.c[I], z4)), mfma16(vr[S], d.s[I], mfma16(vi[S], d.c[I], z4)));
      sfor<2>([&](auto C) { *(LAS v2u*)(ua[S] + I * 16 * S_UP + C * 32) = pack4(mfma16(asbf(mf[S][C]), y, z4)); });
    });
  });
}
struct F64 { bf16x8 c[2][2], s[2][2], n[2][2]; };
template <int NU> DI void p2_units(const F64& f, const f32x4 (&wr)[2][2][2], const f32x4 (&wi)[2][2][2], u32 ra, u32 wa) {
  const f32x4 z4 = {0.f, 0.f, 0.f, 0.f};
  bf16x8 vr[NU][2], vi[NU][2], pr[NU][2], pi[NU][2];
  sfor<2 * NU>([&](auto J) {
    constexpr int S = J >> 1, P = J & 1;
    const u32 ad = ra + S * 576 + P * S_BP;
    vr[S][P] = cat(trr(ad), trr(ad + 32 * S_BP)); vi[S][P] = cat(trr(ad + 8), trr(ad + 32 * S_BP + 8));
  });
  CFENCE();
  sfor<2 * NU>([&](auto J) {
    constexpr int S = J >> 1, P = J & 1;
    const f32x4 er = mfma16(f.s[P][0], vi[S][0], mfma16(f.c[P][0], vr[S][0], z4)), ei = mfma16(f.n[P][0], vr[S][0], mfma16(f.c[P][0], vi[S][0], z4));
    const f32x4 od = mfma16(f.s[P][1], vi[S][1], mfma16(f.c[P][1], vr[S][1], z4)), oi = mfma16(f.n[P][1], vr[S][1], mfma16(f.c[P][1], vi[S][1], z4));
    const f32x4 ar = er + od, ai = ei + oi, br = er - od, bi = ei - oi;
    pr[S][P] = pack44(ar * wr[S][P][0] - ai * wi[S][P][0], br * wr[S][P][1] - bi * wi[S][P][1]);
    pi[S][P] = pack44(ar * wi[S][P][0] + ai * wr[S][P][0], br * wi[S][P][1] + bi * wr[S][P][1]);
  });
  sfor<2 * NU>([&](auto J) {
    constexpr int S = J >> 1, P = J & 1;
    const f32x4 er = mfma16(pi[S][0], f.n[P][0], mfma16(pr[S][0], f.c[P][0], z4)), ei = mfma16(pr[S][0], f.s[P][0], mfma16(pi[S][0], f.c[P][0], z4));
    const u32 ad = wa + S * 576 + P * S_BP;
    const f32x4 od = mfma16(pi[S][1], f.n[P][1], mfma16(pr[S][1], f.c[P][1], z4)), oi = mfma16(pr[S][1], f.s[P][1], mfma16(pi[S][1], f.c[P][1], z4));
    *(LAS bf16x8*)ad = pack44(er + od, ei + oi);
    *(LAS bf16x8*)(ad + 32 * S_BP) = pack44(er - od, ei - oi);
  });
}

DI void spc_phase(Ctx& c, u32 raw, u32& epoch, v4u (&wd)[30]) {
  const int l = c.l, w = c.w, q = l >> 4, r = l & 15, qd = c.xcd >> 1, rr = l >> 2;
  const int si = c.xcd * 8 + (c.idx & 7), hf = si & 1;
  const rsrc_t rt = mkrs(&TABLES, T_END);
  const int it = si * 8 + w, cj = (c.xcd & 1) * 8 + (c.idx & 7);
  v4u dv[4];
  {
    const u32 vo = it < 480 ? (u32)(((it / 10) * 32 + (l >> 3)) * 1280 + (it % 10) * 128 + (l & 7) * 16) : OOB;
    sfor<4>([&](auto I) { BLD(dv[I], vo, mkrs(c.wd, 1536 * 1280), I * 8 * 1280); });
  }
  const rsrc_t rf = mkrs(w < 4 ? c.wr : c.wi, 2048 * 642);
  const u32 fo = ((((si >> 1) + 32 * (16 * (w & 3) + q)) * 642) & ~15u) + r * 16, fo2 = r < 9 ? fo : OOB;
  auto f_load = [&](auto J) {
    const u32 p = (J / 3) ^ (2 * hf);
    DMA(S_W + (4 * w + p) * S_WQ + (p >> 1) * 4 + (J % 3) * 1024, J % 3 < 2 ? fo : fo2, rf, p * 128 * 642 + (J % 3) * 256);
  };
  __builtin_amdgcn_s_barrier();
  sfor<3>([&](auto J) { f_load(J); });
  tag_wait(c, raw, epoch);
  if (w < 4) d32_make(l, w, si, true, S_DI);
  asm volatile("s_waitcnt vmcnt(3)" : "+v"(dv[0]), "+v"(dv[1]), "+v"(dv[2]), "+v"(dv[3])::"memory");
  sfor<3>([&](auto J) { f_load(IC(J + 3)); });
  {
    v4u gd[4];
    tile_tr(c, dv, gd, S_SCR + w * 2048);
    sfor<4>([&](auto J) {
      const int c16 = (it % 10) * 4 + J;
      st128<SC1>(gd[J], c.rws, it < 480 ? WS_WD + (((c16 / 5) * 48 + it / 10) * 5 + c16 % 5) * 1024 + l * 16 : OOB);
    });
  }
  sfor<6>([&](auto J) { f_load(IC(J + 6)); });
  DMA(S_FI + w * 1024, l * 16, rt, T_F + w * 1024);
  STAMP(3);
  WAITBAR<0>();
  if (w == 6) raise(c, F_W + si);
  STAMP(4);
  const bool two = w < 3 - hf;
  f32x4 wr[2][2][2], wi[2][2][2];
  sfor<2>([&](auto S) {
    if (S == 0 || two) {
      const int kk = col_k(11 * hf + w + 8 * S, r);
      const float sc = ((kk == 0 || kk == 320) ? 1.0f : 2.0f) / (2048.0f * 640.0f);
      const u32 by = 2 * kk + (si & 14);
      u32 ad = S_W + 2 * q * S_WQ + 4 * (q & 1) + (by >> 8) * 1024 + (by & 255), ad2 = ad + 16 * S_WQ;
      OPAQUE(ad2);
      sfor<16>([&](auto E) {
        constexpr int p = E >> 3, b = (E >> 2) & 1, e = E & 3, off = ((e >> 1) + 8 * b) * S_WQ + (2 * (e & 1) + p) * 256;
        wr[S][p][b][e] = bflo(*(LAS u16*)(ad + off));
        wi[S][p][b][e] = bflo(*(LAS u16*)(ad2 + off));
      });
      sfor<4>([&](auto E) { wr[S][E >> 1][E & 1] *= sc; wi[S][E >> 1][E & 1] *= sc; PIN(wr[S][E >> 1][E & 1]); PIN(wi[S][E >> 1][E & 1]); });
    }
  });
  F64 f;
  sfor<4>([&](auto P) {
    f.c[P >> 1][P & 1] = *(LAS bf16x8*)(S_FI + P * 1024 + l * 16);
    f.s[P >> 1][P & 1] = *(LAS bf16x8*)(S_FI + 4096 + P * 1024 + l * 16);
    f.n[P >> 1][P & 1] = neg8(f.s[P >> 1][P & 1]);
    PIN(f.n[P >> 1][P & 1]);
  });
  const int sl0 = w + (w >= (hf ? 3 : 7));
  u32 bb = S_W + (8 * w + q) * S_BP + r * 16, ra = S_W + sl0 * 64 + 2 * rr * S_BP + (l & 3) * 16, wa = S_W + sl0 * 64 + 2 * r * S_BP + q * 16;
  u32 zi = WS_Z + (si >> 1) * 98304 + hf * 768 + (8 * w + q) * 1536 + r * 16;
  u32 yo = WS_Y + (si >> 1) * 1536 + hf * 768 + (8 * w + q) * 49152 + r * 16;
  OPAQUE(bb); OPAQUE(ra); OPAQUE(wa); OPAQUE(zi); OPAQUE(yo);
  STAMP(6);
  if (w >= 6) poll_flags(c, F_Z + 64 * (hf + w - 6) + l);
  WAITBAR<63>();
  STAMP(7);
  v4u sf[3][2], mf[3][2];
  {
    v4u y[6];
    sfor<6>([&](auto I) { y[I] = ld128<SC1>(c.rws, zi + (I % 3) * 256, (I / 3) * 4 * 1536); });
    GAP();
    sfor<6>([&](auto I) { *(LAS v4u*)(bb + (I / 3) * 4 * S_BP + (I % 3) * 256) = y[I]; });
  }
  WAITBAR<63>();
  STAMP(8);
  v4u cv[16];
  if (two) p2_units<2>(f, wr, wi, ra, wa);
  else {
    p2_units<1>(f, wr, wi, ra, wa);
    CFENCE();
    const rsrc_t rw = mkrs(cj >= 6 ? c.wu : c.wg, 640 * 3072);
    const u32 vo = cj < 12 && w >= 3 ? (u32)((128 * (w - 3) + (l >> 3)) * 3072 + (384 * qd + 64 * (cj % 6)) * 2 + (l & 7) * 16) : OOB;
    sfor<16>([&](auto I) { cv[I] = ld128(rw, vo, I * 8 * 3072); });
  }
  WAITBAR<63>();
  STAMP(9);
  sfor<6>([&](auto I) { st128<SC1>(*(LAS v4u*)(bb + (I / 3) * 4 * S_BP + (I % 3) * 256), c.rws, yo + (I % 3) * 256 + (I / 3) * 4 * 49152); });
  CFENCE();
  if (w >= 3) {
    u32 k10 = 0x00003f80u, k01 = 0x3f800000u;
    asm volatile("" : "+s"(k10), "+s"(k01));
    float t[8] = {0.f, 0.f, 0.f, 0.f, 0.f, 0.f, 0.f, 0.f};
    sfor<64>([&](auto E) { constexpr int i = E / 4, e = E % 4; t[2 * e] = dot2(k10, cv[i][e], t[2 * e]); t[2 * e + 1] = dot2(k01, cv[i][e], t[2 * e + 1]); });
#pragma unroll
    for (int e = 0; e < 4; ++e) t[e] = fold32(t[e], t[e + 4]);
    t[0] = fold16(t[0], t[2]); t[1] = fold16(t[1], t[3]);
    const bool up = l & 8;
    *(LAS float*)(S_CS + w * 256 + (8 * (l & 7) + (l >> 3)) * 4) = (up ? t[1] : t[0]) + dpp<0x128>(up ? t[0] : t[1]);
  }
  WAITBAR<0>();
  if (w == 6) raise(c, F_Y + si);
  STAMP(10);
  if (w == 5) {
    float s = 0.f;
    sfor<5>([&](auto I) { s += *(LAS float*)(S_CS + (I + 3) * 256 + l * 4); });
    st64<SC1>(v2u{__float_as_uint(s), c.tag}, c.rws, cj < 12 ? WS_CS + ((cj / 6) * 1536 + 384 * qd + 64 * (cj % 6) + l) * 8 : OOB);
  }
  sfor<6>([&](auto J) { mf[J >> 1][J & 1] = ld128(rt, (J < 4 || w < 5) ? l * 16 : OOB, (w + 8 * (J >> 1)) * 4096 + (2 + (J & 1)) * 1024); });
  sfor<6>([&](auto J) { sf[J >> 1][J & 1] = ld128(rt, l * 16, T_S + 6144 + J * 1024); });
  CFENCE();
  D32 d;
  d32_read(d, l, S_DI);
  PIN(d.n[0]); PIN(d.n[1]);
  u32 yi = WS_Y + si * 49152 + (4 * w + q) * 1536 + r * 16, cw = S_W + (4 * w + q) * S_CP + r * 16;
  u32 ur = S_U + 4 * w * S_UP + l * 8, ut = S_U + 4 * w * S_UP + 1024 + (l < 40 ? l : (l & 7) + 32) * 8;
  u32 ao = WS_A + (64 * (4 * w + (q & 1)) + si) * 1280 + r * 32 + (q >> 1) * 16, ao2 = r < 8 ? ao : OOB, ga[3], gb[3];
  sfor<3>([&](auto S) { const int g = w + 8 * S; ga[S] = S_W + rr * S_CP + (g + g / 7) * 64 + (l & 3) * 16; gb[S] = S_U + r * S_UP + g * 64 + q * 8; OPAQUE(ga[S]); OPAQUE(gb[S]); });
  OPAQUE(yi); OPAQUE(cw); OPAQUE(ur); OPAQUE(ut); OPAQUE(ao); OPAQUE(ao2);
  if (w == 7) poll_flags(c, F_Y + l);
  WAITBAR<63>();
  STAMP(11);
  {
    v4u y[6];
    sfor<6>([&](auto I) { y[I] = ld128<SC1>(c.rws, yi + I * 256); });
    GAP();
    __builtin_amdgcn_s_barrier();
    sfor<6>([&](auto I) { wd_load<I>(c, wd); });
    GAP();
    sfor<6>([&](auto I) { *(LAS v4u*)(cw + I * 256) = y[I]; });
  }
  WAITBAR<63>();
  STAMP(12);
  if (w < 5) p3_groups<3>(d, mf, ga, gb);
  else p3_groups<2>(d, mf, ga, gb);
  GAP();
  sfor<6>([&](auto I) { wd_load<6 + I>(c, wd); });
  WAITBAR<63>();
  STAMP(13);
  sfor<2>([&](auto Z) {
    f32x4 o[2][3];
    sfor<2>([&](auto Y) { stage40<3>(ur + (2 * Z + Y) * S_UP, ut + (2 * Z + Y) * S_UP, sf, o[Y]); });
    sfor<3>([&](auto T) {
      const v2u a = pack4(o[0][T]), b = pack4(o[1][T]);
      const auto x = __builtin_amdgcn_permlane16_swap(a.x, b.x, false, false), y = __builtin_amdgcn_permlane16_swap(a.y, b.y, false, false);
      st128<SC1>(v4u{x[0], y[0], x[1], y[1]}, c.rws, (T < 2 ? ao : ao2) + T * 512 + Z * 128 * 1280);
    });
  });
  WAITBAR<63>();
  STAMP(14);
  sfor<WDL - 12>([&](auto I) { wd_load<12 + I>(c, wd); });
  arrive_publish<WDL - 12>(c, C_ARRA, F_A + si);
  STAMP(15);
}

constexpr u32 OUT_A = 0, OUT_B = 61440, OUT_S = 122880, OUT_O = OUT_S + 20480, OUT_OP = 176;
DI void out_phase(const Ctx& c, v4u (&wd)[30], u32 epoch) {
  const int l = c.l, w = c.w, q = l >> 4, r = l & 15, a = w & 3;
  const int rt = 4 * c.xcd + (c.idx & 3), ct = c.idx >> 2;
  constexpr int HW = 2;
  u32 ho[4];
  sfor<4>([&](auto I) { ho[I] = WS_H + ((rt * 4 + ((a + I) & 3)) * 48 + 6 * out_kw(c)) * 1024; });
  v4u hq[HW][4];
  if (w == 7) flag_wait(c, true, C_GOH, F_H + 12 * (rt >> 1) + (l < 12 ? l : 0));
  sfor<30 - WDL>([&](auto I) { wd_load<WDL + I>(c, wd); });
  CFENCE();
  if (w != 7) flag_wait(c, false, C_GOH, 0);
  STAMP(16);
  if (w < 4) __builtin_amdgcn_s_setprio(1);
  sfor<HW>([&](auto I) { sfor<4>([&](auto ST) { hq[I][ST] = ld128<SC1>(c.rws, l * 16, ho[ST] + I * 1024); }); });
  GAP();
  f32x4 acc[5][4];
  sfor<6>([&](auto I) {
    constexpr int i = I;
    sfor<4>([&](auto ST) {
      sfor<5>([&](auto T) { acc[T][ST] = mfma16(asbf(wd[5 * i + T]), asbf(hq[i % HW][ST]), i == 0 ? f32x4{0.f, 0.f, 0.f, 0.f} : acc[T][ST]); });
      if constexpr (i + HW < 6) hq[i % HW][ST] = ld128<SC1>(c.rws, l * 16, ho[ST] + (i + HW) * 1024);
      GAP();
    });
  });
  STAMP(17);
  __builtin_amdgcn_s_setprio(0);
  const u32 pb = (w < 4 ? OUT_A : OUT_B) + l * 16;
  sfor<3>([&](auto I) { sfor<5>([&](auto T) { *(LAS f32x4*)(pb + (((a + I + 1) & 3) * 3 + I) * 5120 + T * 1024) = acc[T][I + 1]; }); });
  if (w < 4) {
    if (w == 0) poll_flags(c, F_A + l);
    group_barrier(c, C_OUTA, 4);
    STAMP(18);
    v4u xs[3], as[3];
    u32 vo[3], lo[3];
    sfor<3>([&](auto I) {
      const u32 ch = 64 * I + l, row = (ch * 6554u) >> 16, cc = ch - 10 * row;
      vo[I] = ch < 160 ? (rt * 64 + a * 16 + row) * 1280 + ct * 160 + cc * 16 : OOB;
      lo[I] = OUT_O + (a * 16 + (row & 15)) * OUT_OP + cc * 16;
      xs[I] = ld128(c.rx, vo[I]); as[I] = ld128<SC1>(c.rws, vo[I], WS_A);
    });
    GAP();
    sfor<5>([&](auto T) {
      f32x4 s = acc[T][0];
      sfor<3>([&](auto J) { s += *(LAS f32x4*)(pb + (a * 3 + J) * 5120 + T * 1024); });
      *(LAS f32x4*)(OUT_S + (a * 5 + T) * 1024 + l * 16) = s;
    });
    STAMP(19);
    WAITBAR<63>();
    sfor<12>([&](auto E) { constexpr int i = E / 4, e = E % 4; xs[i][e] = cvtpk(bflo(xs[i][e]) + bflo(as[i][e]), bfhi(xs[i][e]) + bfhi(as[i][e])); });
    sfor<3>([&](auto I) { PIN(xs[I]); });
    WAITBAR<63>();
    STAMP(20);
    const rsrc_t ro = mkrs(c.out, 2048 * 1280);
    sfor<3>([&](auto I) {
      const v4u b = *(LAS v4u*)lo[I];
      v4u o;
      sfor<4>([&](auto E) { o[(int)E] = cvtpk(bflo(xs[I][(int)E]) + bflo(b[(int)E]), bfhi(xs[I][(int)E]) + bfhi(b[(int)E])); });
      st128<SC1>(o, ro, vo[I]);
    });
  } else {
    STAMP(19);
    WAITBAR<63>();
    sfor<5>([&](auto T) {
      f32x4 s = acc[T][0] + *(LAS f32x4*)(OUT_S + (a * 5 + T) * 1024 + l * 16);
      sfor<3>([&](auto J) { s += *(LAS f32x4*)(pb + (a * 3 + J) * 5120 + T * 1024); });
      *(LAS v2u*)(OUT_O + (a * 16 + r) * OUT_OP + T * 32 + q * 8) = pack4(s);
    });
    WAITBAR<63>();
    if (w == 4 && blockIdx.x == 0) {
      if (l == 0) st32<SC1>(epoch, c.rws, 0);
    }
  }
}

__global__ __launch_bounds__(512, 2) void k_all(const u16* x, const u16* wr, const u16* wi, const u16* wg, const u16* wu, const u16* wd, u16* out, char* ws) {
  Ctx c;
  c.ws = ws;
  c.rx = mkrs(x, 2048 * 1280);
  c.rws = mkrs(ws, WS_END);
  c.wr = wr; c.wi = wi; c.wg = wg; c.wu = wu; c.wd = wd; c.out = out;
  c.l = threadIdx.x & 63;
  c.w = __builtin_amdgcn_readfirstlane(threadIdx.x >> 6);
  c.xcd = blockIdx.x & 7;
  c.idx = blockIdx.x >> 3;
  u32 raw, epoch;
  asm volatile("s_load_dword %0, %1, 0x0" : "=s"(raw) : "s"(ws) : "memory");
  if (threadIdx.x < C_N) *(LAS u32*)(c.lb + L_CTL + threadIdx.x * 4) = 0;
  STAMP(0);
  const bool spc = (c.idx >> 3) == 3;
  v4u wdf[30];
  if (spc) spc_phase(c, raw, epoch, wdf);
  else glu_phase(c, raw, epoch, wdf);
  GAP();
  out_phase(c, wdf, epoch);
  STAMP(21);
}

extern "C" void kernel_launch(void* const* d_in, const int* in_sizes, int n_in, void* d_out, int out_size, void* d_ws, size_t ws_size, hipStream_t stream) {
  (void)hipFuncSetAttribute((const void*)k_all, hipFuncAttributeMaxDynamicSharedMemorySize, LDS_BYTES);
  k_all<<<256, 512, LDS_BYTES, stream>>>((const u16*)d_in[0], (const u16*)d_in[1], (const u16*)d_in[2], (const u16*)d_in[4], (const u16*)d_in[5],
                                         (const u16*)d_in[6], (u16*)d_out, (char*)d_ws);
}
```

```cpp
#include <hip/hip_runtime.h>
#include <utility>

typedef unsigned u32;
typedef unsigned short u16;
typedef unsigned long long u64;
typedef unsigned v4u __attribute__((ext_vector_type(4)));
typedef unsigned v2u __attribute__((ext_vector_type(2)));
typedef float f32x2 __attribute__((ext_vector_type(2)));
typedef float f32x4 __attribute__((ext_vector_type(4)));
typedef float f32x16 __attribute__((ext_vector_type(16)));
typedef __bf16 bf16x2 __attribute__((ext_vector_type(2)));
typedef __bf16 bf16x4 __attribute__((ext_vector_type(4)));
typedef __bf16 bf16x8 __attribute__((ext_vector_type(8)));
typedef __amdgpu_buffer_rsrc_t rsrc_t;
#define LAS __attribute__((address_space(3)))
#define DI __device__ __forceinline__
#define UNLIKELY(x) __builtin_expect(!!(x), 0)
#define GAP() __builtin_amdgcn_sched_barrier(0)
#define IC(x) std::integral_constant<int, (x)>{}
#pragma clang diagnostic ignored "-Wint-to-pointer-cast"

template <class F, int... Is> DI void sfor_(F&& f, std::integer_sequence<int, Is...>) { (f(std::integral_constant<int, Is>{}), ...); }
template <int N, class F> DI void sfor(F&& f) { sfor_(f, std::make_integer_sequence<int, N>{}); }

constexpr u32 WS_STAT = 0x4000;
constexpr u32 WS_CS = 0x8000;
constexpr u32 WS_FLAG = 0x20000, F_Z = 0, F_H = 192, F_W = 384, F_A = 448, F_Y = 512;
constexpr u32 WS_Z = 0x40000;
constexpr u32 WS_Y = WS_Z + 0x300000;
constexpr u32 WS_WD = WS_Y + 0x300000;
constexpr u32 WS_H = WS_WD + 0x1E0000;
constexpr u32 WS_A = WS_H + 0x600000;
constexpr u32 WS_END = WS_A + 0x280000;
constexpr u32 OOB = 0x40000000u;
constexpr int SC1 = 16;
constexpr u32 CODE_LINES = 226;
constexpr u32 SPIN_CAP = 1u << 16;
constexpr u32 LDS_BYTES = 163840, SLOTB = 16384;
constexpr u32 L_CTL = LDS_BYTES - 256;

DI u32 cvtpk(float lo, float hi) { f32x2 v = {lo, hi}; return __builtin_bit_cast(u32, __builtin_convertvector(v, bf16x2)); }
DI float bflo(u32 v) { return __uint_as_float(v << 16); }
DI float bfhi(u32 v) { return __uint_as_float(v & 0xffff0000u); }
DI float rbf(float f) { return bflo(cvtpk(f, f)); }
DI bf16x8 asbf(v4u v) { return __builtin_bit_cast(bf16x8, v); }
DI v4u asu(bf16x8 v) { return __builtin_bit_cast(v4u, v); }
DI rsrc_t mkrs(const void* p, u32 n) { return __builtin_amdgcn_make_buffer_rsrc((void*)p, 0, n, 0x00020000); }
DI void DMA(u32 m0v, u32 voff, rsrc_t rs, u32 soff) {
  asm volatile("s_mov_b32 m0, %0\n\ts_nop 0\n\tbuffer_load_dwordx4 %1, %2, %3 offen lds" ::"s"(m0v), "v"(voff), "s"(rs), "s"(soff) : "memory");
}
DI void BLD(v4u& d, u32 voff, rsrc_t rs, u32 soff) { asm volatile("buffer_load_dwordx4 %0, %1, %2, %3 offen" : "=v"(d) : "v"(voff), "s"(rs), "s"(soff) : "memory"); }
DI void BLD32(u32& d, u32 voff, rsrc_t rs) { asm volatile("buffer_load_dword %0, %1, %2, 0 offen sc1" : "=v"(d) : "v"(voff), "s"(rs) : "memory"); }
template <int M0C, int SOC> DI void DMA2(u32 m0a, u32 v0, u32 v1, rsrc_t rs) {
  u32 t;
  asm volatile("s_add_u32 m0, %1, %5\n\ts_mov_b32 %0, %6\n\tbuffer_load_dwordx4 %2, %4, %0 offen lds\n\tbuffer_load_dwordx4 %3, %4, %0 offen offset:1024 lds"
               : "=&s"(t) : "s"(m0a), "v"(v0), "v"(v1), "s"(rs), "i"(M0C), "i"(SOC) : "memory", "scc");
}
template <int N> DI void WAITBAR() { asm volatile("s_waitcnt vmcnt(%0) lgkmcnt(0)\n\ts_barrier" ::"i"(N) : "memory"); }
DI void DRAIN() { asm volatile("s_waitcnt vmcnt(0)" ::: "memory"); }
DI void OPAQUE(u32& v) { asm volatile("" : "+v"(v)); }
DI void PIN(bf16x8& v) { asm volatile("" : "+v"(v)); }
DI void PIN(v4u& v) { asm volatile("" : "+v"(v)); }
DI void PIN(f32x4& v) { asm volatile("" : "+v"(v)); }
DI void PIN(float& v) { asm volatile("" : "+v"(v)); }
DI void CFENCE() { asm volatile("" ::: "memory"); }
DI bf16x4 trr(u32 a) { return __builtin_amdgcn_ds_read_tr16_b64_v4bf16((LAS bf16x4*)a); }
DI bf16x8 cat(bf16x4 a, bf16x4 b) { return __builtin_shufflevector(a, b, 0, 1, 2, 3, 4, 5, 6, 7); }
DI f32x4 mfma16(bf16x8 a, bf16x8 b, f32x4 c) { return __builtin_amdgcn_mfma_f32_16x16x32_bf16(a, b, c, 0, 0, 0); }
DI f32x16 mfma32(bf16x8 a, bf16x8 b, f32x16 c) { return __builtin_amdgcn_mfma_f32_32x32x16_bf16(a, b, c, 0, 0, 0); }
template <int AUX = 0> DI v4u ld128(rsrc_t rs, u32 voff, u32 soff = 0) { return __builtin_amdgcn_raw_buffer_load_b128(rs, voff, soff, AUX); }
template <int AUX = 0> DI v2u ld64(rsrc_t rs, u32 voff, u32 soff = 0) { return __builtin_amdgcn_raw_buffer_load_b64(rs, voff, soff, AUX); }
template <int AUX = 0> DI u32 ld32(rsrc_t rs, u32 voff, u32 soff = 0) { return __builtin_amdgcn_raw_buffer_load_b32(rs, voff, soff, AUX); }
template <int AUX = 0> DI void st128(v4u v, rsrc_t rs, u32 voff, u32 soff = 0) { __builtin_amdgcn_raw_buffer_store_b128(v, rs, voff + soff, 0, AUX); }
template <int AUX = 0> DI void st64(v2u v, rsrc_t rs, u32 voff, u32 soff = 0) { __builtin_amdgcn_raw_buffer_store_b64(v, rs, voff, soff, AUX); }
template <int AUX = 0> DI void st32(u32 v, rsrc_t rs, u32 voff, u32 soff = 0) { __builtin_amdgcn_raw_buffer_store_b32(v, rs, voff, soff, AUX); }
DI float cosr(float rev) { return __builtin_amdgcn_cosf(rev); }
template <int CTRL> DI float dpp(float v) { return __int_as_float(__builtin_amdgcn_update_dpp(0, __float_as_int(v), CTRL, 0xf, 0xf, true)); }
DI float fold32(float a, float b) {
  const auto r = __builtin_amdgcn_permlane32_swap(__float_as_uint(a), __float_as_uint(b), false, false);
  return __uint_as_float(r[0]) + __uint_as_float(r[1]);
}
DI float fold16(float a, float b) {
  const auto r = __builtin_amdgcn_permlane16_swap(__float_as_uint(a), __float_as_uint(b), false, false);
  return __uint_as_float(r[0]) + __uint_as_float(r[1]);
}

struct Ctx {
  rsrc_t rx, rws;
  const u16 *wr, *wi, *wg, *wu, *wd;
  u16* out;
  u32 tag;
  int l, w, xcd, idx;
};

DI void raise(const Ctx& c, u32 flag) { if (c.l == 0) st32<SC1>(c.tag, c.rws, WS_FLAG + flag * 128); }
DI void arrive_publish(const Ctx& c, u32 flag) {
  DRAIN();
  u32 old = 0;
  if (c.l == 0) old = __hip_atomic_fetch_add((LAS u32*)L_CTL, 1u, __ATOMIC_RELAXED, __HIP_MEMORY_SCOPE_WORKGROUP);
  old = __builtin_amdgcn_readfirstlane(old);
  if (old == 7u) raise(c, flag);
}
DI void poll_flags(const Ctx& c, u32 flag) {
#pragma nounroll
  for (u32 i = 0; i < SPIN_CAP && ld32<SC1>(c.rws, WS_FLAG + flag * 128) != c.tag; ++i) __builtin_amdgcn_s_sleep(2);
  CFENCE();
}
DI void tag_wait(Ctx& c, u32 raw, u32& epoch) {
  asm volatile("s_waitcnt lgkmcnt(0)\n\ts_add_u32 %0, %1, 1" : "=s"(epoch) : "s"(raw) : "scc");
  c.tag = (epoch * 0x9E3779B1u) ^ 0x5bd1e995u;
}
constexpr int NPG = 5;
DI int out_st(const Ctx& c) { return 16 * c.xcd + NPG * (c.idx >> 3); }
DI int out_ct(const Ctx& c) { return c.idx & 7; }
DI u32 out_ks(const Ctx& c, int i) {
  const int k = i + (2 * (c.idx >> 3) + 3 * ((c.idx >> 2) & 1)) % 6;
  return 6 * c.w + (k >= 6 ? k - 6 : k);
}
template <int I> DI void wd_load(const Ctx& c, v4u (&wd)[30]) {
  wd[I] = ld128<SC1>(c.rws, c.l * 16, WS_WD + ((out_ct(c) * 48 + out_ks(c, I / 5)) * 5 + I % 5) * 1024);
}

DI bf16x8 pack8(const float (&v)[8]) { return asbf(v4u{cvtpk(v[0], v[1]), cvtpk(v[2], v[3]), cvtpk(v[4], v[5]), cvtpk(v[6], v[7])}); }
DI bf16x8 pack44(f32x4 a, f32x4 b) { return asbf(v4u{cvtpk(a[0], a[1]), cvtpk(a[2], a[3]), cvtpk(b[0], b[1]), cvtpk(b[2], b[3])}); }
DI v2u pack4(f32x4 a) { return v2u{cvtpk(a[0], a[1]), cvtpk(a[2], a[3])}; }
DI bf16x8 neg8(bf16x8 v) { return asbf(asu(v) ^ 0x80008000u); }
DI float dot2(u32 a, u32 b, float c) { return __builtin_amdgcn_fdot2_f32_bf16(__builtin_bit_cast(bf16x2, a), __builtin_bit_cast(bf16x2, b), c, false); }
DI void tile_tr(const Ctx& c, const v4u (&v)[4], v4u (&f)[4], u32 buf) {
  const int l = c.l, rr = 4 * (l >> 4) + ((l & 15) >> 2);
  const u32 wa = buf + (l >> 3) * 128 + (((l & 7) ^ (((l >> 4) & 3) << 1)) * 16), ra = buf + rr * 128 + (l & 3) * 8;
  bf16x4 t[2][4];
#pragma unroll
  for (int u = 0; u < 2; ++u) {
    *(LAS v4u*)wa = v[2 * u];
    *(LAS v4u*)(wa + 1024) = v[2 * u + 1];
    CFENCE();
#pragma unroll
    for (int j = 0; j < 4; ++j) t[u][j] = trr(ra + ((j ^ ((rr >> 1) & 3)) * 32));
    CFENCE();
  }
#pragma unroll
  for (int j = 0; j < 4; ++j) f[j] = asu(cat(t[0][j], t[1][j]));
}
struct Cos640 { u16 v[640]; };
constexpr Cos640 make_cos() {
  Cos640 t{};
  for (int i = 0; i < 640; ++i) {
    int n = i > 320 ? 640 - i : i;
    const bool neg = n > 160;
    if (neg) n = 320 - n;
    const double x = 6.283185307179586476925 * n / 640.0, x2 = x * x;
    double term = 1.0, sum = 1.0;
    for (int j = 1; j <= 12; ++j) { term *= -x2 / ((2 * j - 1) * (2 * j)); sum += term; }
    const float f = n == 160 ? 0.f : (float)(neg ? -sum : sum);
    const u32 u = __builtin_bit_cast(u32, f);
    t.v[i] = (u16)((u + 0x7fffu + ((u >> 16) & 1u)) >> 16);
  }
  return t;
}
constexpr Cos640 COS = make_cos();
constexpr int col_kc(int g, int m) { return g == 20 ? 320 : (g == 0 ? (m < 8 ? 40 * m : 40 * m - 300) : (g + 40 * m > 320 ? 640 - g - 40 * m : g + 40 * m)); }
constexpr u16 m_at(int g, int cc, int m, int cp, int d2) {
  const bool edge = g == 0 || g == 20, on = !edge || (g == 0 ? (m >> 3) == cp : (m == 0 && cp == 0));
  return on ? COS.v[(col_kc(g, m) * d2 + 160 * cc + ((cp && !edge) ? (g + 40 * m > 320 ? 160 : 480) : 0)) % 640] : (u16)0;
}
struct MGroup { u16 v[4][512]; };
constexpr MGroup make_m(int g) {
  MGroup t{};
  for (int h = 0; h < 2; ++h)
    for (int l = 0; l < 64; ++l)
      for (int e = 0; e < 8; ++e) {
        t.v[h][l * 8 + e] = m_at(g, h, l & 15, l >> 5, 8 * ((l >> 4) & 1) + e);
        const u16 v = m_at(g, e >> 2, 4 * (l >> 4) + (e & 3), h, l & 15);
        t.v[2 + h][l * 8 + e] = ((g == 0 || g == 20) && l < 16 && (e & 3) == 0 && (v & 0x7fff)) ? (u16)(v - 0x80) : v;
      }
  return t;
}
template <int G> constexpr MGroup M_OF = make_m(G);
struct Small { u16 s[6][1024]; u16 f[8][512]; };
constexpr Small make_small() {
  Small t{};
  for (int w = 0; w < 6; ++w)
    for (int l = 0; l < 64; ++l)
      for (int e = 0; e < 12; ++e) {
        const int k = 16 * (e >> 2) + 4 * (l >> 4) + (e & 3), j = w < 3 ? 14 * w + (l & 15) : k, d1 = w < 3 ? k : 16 * (w - 3) + (l & 15);
        const int g = j == 1 ? 20 : (j == 40 ? 0 : j >> 1);
        t.s[w][(e >> 3) * 512 + l * 8 + (e & 7)] = (j <= 40 && d1 < 40) ? COS.v[(16 * g * d1 + (((j & 1) && j != 1) ? 160 : 0)) % 640] : (u16)0;
      }
  for (int w = 0; w < 8; ++w)
    for (int l = 0; l < 64; ++l)
      for (int e = 0; e < 8; ++e)
        t.f[w][l * 8 + e] = COS.v[(10 * (2 * (l & 15) + ((w >> 1) & 1)) * (2 * (4 * (l >> 4) + (e & 3)) + (w & 1) + 32 * (e >> 2)) + (w >= 4 ? 480 : 0)) % 640];
  return t;
}
constexpr Small SMALL = make_small();
struct Tables { MGroup m[21]; Small s; };
template <int... G> constexpr Tables make_tables(std::integer_sequence<int, G...>) { return Tables{{M_OF<G>...}, SMALL}; }
static __device__ __attribute__((aligned(128))) const Tables TABLES = make_tables(std::make_integer_sequence<int, 21>{});
constexpr u32 T_S = 21 * 4096, T_F = T_S + 12288, T_END = T_F + 8192;
static_assert(sizeof(Tables) == T_END, "");

DI int col_k(int g, int m) {
  const int kp = g + 40 * m;
  return g == 20 ? 320 : (g == 0 ? (m < 8 ? kp : kp - 300) : (kp > 320 ? 640 - kp : kp));
}
template <int NT> DI void stage40(u32 a, u32 tail, const v4u (&f)[NT][2], f32x4 (&acc)[NT]) {
  const bf16x8 x0 = cat(trr(a), trr(a + 512)), x1 = cat(trr(tail), trr(tail));
  sfor<NT>([&](auto T) { acc[T] = mfma16(x1, asbf(f[T][1]), mfma16(x0, asbf(f[T][0]), f32x4{0.f, 0.f, 0.f, 0.f})); });
}
struct D32 { bf16x8 c[2], s[2], n[2]; };
DI void d32_make(int l, int i, int sl, bool back, u32 base) {
  const int n = 16 * (i >> 1) + (l & 15);
  const float da = (float)(64 * n + (back ? sl : 0)) * (1.0f / 2048.0f);
  const float a0 = __builtin_fmaf(da, (float)(4 * (l >> 4)), (float)(back ? 0 : n * sl) * (1.0f / 2048.0f) - ((i & 1) ? 0.25f : 0.f));
  float cc[8];
  sfor<8>([&](auto E) { cc[E] = cosr(__builtin_fmaf(da, (float)(16 * (E >> 2) + (E & 3)), a0)) * (back ? 2.0f / (2048.0f * 640.0f) : 1.0f); });
  *(LAS bf16x8*)(base + i * 1024 + l * 16) = pack8(cc);
}
DI void d32_read(D32& d, int l, u32 base) {
  sfor<2>([&](auto U) {
    d.c[U] = *(LAS bf16x8*)(base + 2 * U * 1024 + l * 16); d.s[U] = *(LAS bf16x8*)(base + (2 * U + 1) * 1024 + l * 16);
    d.n[U] = neg8(d.s[U]);
  });
}

constexpr u32 OUT_O = 102400, OUT_OP = 176;
DI void SOPAQUE(u32& v) { asm volatile("" : "+s"(v)); }
template <int NP> struct OutAddr { u32 vo[2], lo[2], hv[NP], pw[NP], hk[6], oa[4], fo, ho; };
template <int NP> DI void out_prep(const Ctx& c, OutAddr<NP>& o) {
  const int l = c.l, w = c.w, st = out_st(c), ct = out_ct(c);
  sfor<(NP > 3 ? 2 : 1)>([&](auto I) {
    const u32 ch = 64 * I + l, row = (ch * 6554u) >> 16, cc = ch - 10 * row;
    o.vo[I] = ch < 20 * NP ? (st * 16 + w * 2 * NP + row) * 1280 + ct * 160 + cc * 16 : OOB;
    o.lo[I] = OUT_O + (ch < 20 * NP ? (w * 2 * NP + row) * OUT_OP + cc * 16 : 80 * OUT_OP + l * 16);
    OPAQUE(o.vo[I]); OPAQUE(o.lo[I]);
  });
  sfor<NP>([&](auto I) {
    const int t = (w + I) % NP;
    o.hv[I] = WS_H + (st + t) * 49152; o.pw[I] = t * 20480 + (w >> 1) * 1024 + (w & 1) * 8;
    SOPAQUE(o.hv[I]); SOPAQUE(o.pw[I]);
  });
  sfor<4>([&](auto J) { const int t = w + 8 * J; o.oa[J] = OUT_O + (t / 5) * 16 * OUT_OP + (t % 5) * 32; SOPAQUE(o.oa[J]); });
  sfor<6>([&](auto K) { o.hk[K] = out_ks(c, K) * 1024; SOPAQUE(o.hk[K]); });
  o.fo = F_A + ((st * 16 + w * 2 * NP + (l & 15) % (2 * NP)) & 63);
  o.ho = F_H + 12 * ((st + (l & 2 ? NP - 1 : 0)) >> 3) + ((6 * w + 5 * (l & 1)) >> 2);
  OPAQUE(o.fo); OPAQUE(o.ho);
}
template <int NP> DI void out_phase(const Ctx& c, u16* out, v4u (&wd)[30], const OutAddr<NP>& o, bool ready) {
  constexpr int NI = NP > 3 ? 2 : 1, NJ = 6 * NP, WIN = NJ < 10 ? NJ : 10;
  const int l = c.l;
  u32 hv[NP], pw[NP];
  sfor<NP>([&](auto I) { hv[I] = o.hv[I] + l * 16; pw[I] = o.pw[I] + l * 16; });
  CFENCE();
  if (!ready) poll_flags(c, o.ho);
  CFENCE();
  v4u hq[NP][6], xs[NI], as[NI];
  sfor<WIN>([&](auto J) { hq[J / 6][J % 6] = ld128<SC1>(c.rws, hv[J / 6], o.hk[J % 6]); });
  u32 fa;
  if constexpr (NP == 1) { fa = ld32<SC1>(c.rws, WS_FLAG + o.fo * 128); xs[0] = ld128(c.rx, o.vo[0]); }
  GAP();
  f32x4 acc[NP][5];
  bool late = true;
  auto a_load = [&]() { CFENCE(); sfor<NI>([&](auto I) { as[I] = ld128<SC1>(c.rws, o.vo[I], WS_A); }); };
  sfor<NJ>([&](auto J) {
    constexpr int i = J / 6, k = J % 6, n = J + WIN;
    sfor<5>([&](auto T) { acc[i][T] = mfma16(asbf(wd[5 * k + T]), asbf(hq[i][k]), k == 0 ? f32x4{0.f, 0.f, 0.f, 0.f} : acc[i][T]); });
    if constexpr (n < NJ) hq[n / 6][n % 6] = ld128<SC1>(c.rws, hv[n / 6], o.hk[n % 6]);
    if constexpr (i > 0 && k < 5) *(LAS v2u*)(pw[i - 1] + k * 4096) = pack4(acc[i - 1][k]);
    if constexpr (NP > 1 && J == NJ - 8) fa = ld32<SC1>(c.rws, WS_FLAG + o.fo * 128);
    if constexpr (NP > 1 && J >= NJ - 10 && J < NJ - 10 + NI) xs[J - NJ + 10] = ld128(c.rx, o.vo[J - NJ + 10]);
    GAP();
  });
  sfor<5>([&](auto T) { *(LAS v2u*)(pw[NP - 1] + T * 4096) = pack4(acc[NP - 1][T]); });
  late = !__all(fa == c.tag);
  if (!late) a_load();
  const u32 one = ((l & 15) >> 2) == (l >> 4) ? ((l & 1) ? 0x3f800000u : 0x00003f80u) : 0u, s0 = (l & 2) ? 0u : one, s1 = (l & 2) ? one : 0u;
  const bf16x8 sel = asbf(v4u{s0, s1, s0, s1});
  const u32 pr = c.w * 4096 + l * 16, ol = (l & 15) * OUT_OP + (l >> 4) * 8;
  WAITBAR<63>();
  auto sum = [&](auto J0, auto N) {
    bf16x8 p[N][4];
    sfor<N>([&](auto J) {
      sfor<4>([&](auto S) { p[J][S] = *(LAS bf16x8*)(pr + (J0 + J) * 32768 + S * 1024); });
    });
    CFENCE();
    sfor<N>([&](auto J) {
      f32x4 t = {0.f, 0.f, 0.f, 0.f};
      sfor<4>([&](auto S) { t = mfma16(sel, p[J][S], t); });
      *(LAS v2u*)(o.oa[J0 + J] + ol) = pack4(t);
    });
  };
  constexpr int NF = 5 * NP / 8;
  if (c.w < 5 * NP - 8 * NF) sum(IC(0), IC(NF + 1));
  else if constexpr (NF > 0) sum(IC(0), IC(NF));
  if (UNLIKELY(late)) { poll_flags(c, o.fo); a_load(); }
  sfor<4 * NI>([&](auto E) { constexpr int i = E / 4, e = E % 4; xs[i][e] = cvtpk(bflo(xs[i][e]) + bflo(as[i][e]), bfhi(xs[i][e]) + bfhi(as[i][e])); });
  sfor<NI>([&](auto I) { PIN(xs[I]); });
  WAITBAR<63>();
  const rsrc_t ro = mkrs(out, 2048 * 1280);
  sfor<NI>([&](auto I) { as[I] = *(LAS v4u*)o.lo[I]; });
  sfor<NI>([&](auto I) {
    v4u r;
    sfor<4>([&](auto E) { r[(int)E] = cvtpk(bflo(xs[I][(int)E]) + bflo(as[I][(int)E]), bfhi(xs[I][(int)E]) + bfhi(as[I][(int)E])); });
    st128(r, ro, o.vo[I]);
  });
}

constexpr int NXS = 4, NWS = 5, PRE = 5;
constexpr bool qIsX(int q) { return q % 3 == 0; }
constexpr int qIdx(int q) { return q % 3 == 0 ? q / 3 : 2 * (q / 3) + (q % 3) - 1; }
constexpr int qSlotOff(int q) { return qIsX(q) ? (qIdx(q) % NXS) * SLOTB : (NXS + qIdx(q) % NWS) * SLOTB; }
constexpr int qFree(int q) { return qIsX(q) ? (qIdx(q) < NXS ? 0 : 2 * (qIdx(q) - NXS) + 2) : (qIdx(q) < NWS ? 0 : qIdx(q) - NWS + 1); }
constexpr int needQ(int st) { return 3 * (st / 2) + 1 + (st & 1); }
constexpr int issuedBefore(int st) {
  int n = PRE;
  for (int s = 0; s < st; s++)
    for (int j = 0; j < 2; j++)
      if (n < 30 && qFree(n) <= s) n++;
  return n;
}
template <int N> DI void WAITBARV() { asm volatile("s_waitcnt vmcnt(%0)\n\ts_barrier" ::"i"(N) : "memory"); }
constexpr int WDC[20] = {0, 0, 0, 0, 0, 0, 0, 0, 1, 1, 1, 1, 1, 1, 1, 1, 6, 6, 6, 0};
constexpr int wdSum() { int n = 0; for (int s = 0; s < 20; ++s) n += WDC[s]; return n; }
constexpr int wdFirst() { int s = 0; while (WDC[s] == 0) ++s; return s; }
constexpr int WDL = wdSum(), WD1 = wdFirst();
constexpr int ST_FW = WD1 - 5, ST_CS = 5, ST_SIDE = 16, N_P1 = 3;
constexpr int dmaAt(int st, int g) { return ((g & 3) == 0 && issuedBefore(st) + (g >> 2) < issuedBefore(st + 1)) ? issuedBefore(st) + (g >> 2) : -1; }
constexpr int wdAt(int st, int g) {
  int i = 0;
  for (int s = 0; s < st; ++s) i += WDC[s];
  if (WDC[st] > 4) return 7 - g < WDC[st] ? i + WDC[st] - 8 + g : -1;
  const int k = (7 - g) / 2;
  return (g & 1) && k < WDC[st] ? i + WDC[st] - 1 - k : -1;
}
constexpr int otherAt(int st, int g) {
  return (wdAt(st, g) >= 0) + (st == 1 && g == 1 ? 1 : 0) + (st == ST_FW && g == 2 ? 1 : 0) + (st == ST_CS && g == 6 ? 1 : 0) + (st == ST_SIDE && g == 0 ? 3 : 0);
}
constexpr int opsAfter(int s0, int g0, int st) {
  int n = 0;
  for (int s = s0; s < st; ++s)
    for (int g = s == s0 ? g0 + 1 : 0; g < 8; ++g) n += 2 * (dmaAt(s, g) >= 0) + otherAt(s, g);
  return n;
}
constexpr int waitN(int st) {
  const int e = needQ(st + 1);
  int n = e < PRE ? 2 * (PRE - 1 - e) + (st == 0 ? N_P1 + 3 : 0) + opsAfter(0, -1, st) : 0;
  for (int s = 0; s < st; ++s)
    for (int g = 0; g < 8; ++g)
      if (dmaAt(s, g) == e) n = otherAt(s, g) + opsAfter(s, g, st);
  return n;
}
static_assert(needQ(2) == PRE - 1 && waitN(WD1 - 1) <= opsAfter(ST_FW, 2, WD1 - 1) && issuedBefore(ST_SIDE) == 30 && WDL <= 30, "");
constexpr u32 P_T = 49152, P_V = 147456, P_VP = 464, P_D = 140288, P_CS = P_V;
static_assert(qSlotOff(9) == P_T && issuedBefore(2) <= 9 && qSlotOff(7) == 131072 && issuedBefore(1) <= 7, "the borrowed slots are filled late enough");

DI void glu_phase(Ctx& c, u32 raw) {
  u32 epoch;
  v4u wd[30];
  const int l = c.l, w = c.w, q = l >> 4, r = l & 15;
  const int panel = (c.xcd & 1) * 8 + (c.idx & 7), sub = c.idx >> 3, grp = (c.xcd >> 1) * 3 + sub, sl = c.xcd * 8 + (c.idx & 7);
  const f32x4 z4 = {0.f, 0.f, 0.f, 0.f};
  const rsrc_t rt = mkrs(&TABLES, T_END);
  v4u xv[5], sf[1][2], mf[2];
  sfor<5>([&](auto I) { BLD(xv[I], (64 * (4 * w + q) + sl) * 1280 + r * 16, c.rx, I * 256); });
  sfor<2>([&](auto I) { BLD(sf[0][I], l * 16, rt, T_S + sub * 2048 + I * 1024); });
  sfor<2>([&](auto I) { BLD(mf[I], w < 7 ? l * 16 : OOB, rt, (7 * sub + w) * 4096 + I * 1024); });
  const int cj = (c.xcd & 1) * 24 + c.idx, cc = cj % 12;
  v4u cv[3];
  auto cs_issue = [&]() {
    const u32 cvo = (160 * (cj / 12) + 20 * w + (l >> 3)) * 3072 + (384 * (c.xcd >> 1) + 64 * (cc % 6)) * 2 + (l & 7) * 16;
    const rsrc_t rc = mkrs(cc >= 6 ? c.wu : c.wg, 640 * 3072);
    BLD(cv[0], cvo, rc, 0); BLD(cv[1], cvo, rc, 8 * 3072); BLD(cv[2], l < 32 ? cvo : OOB, rc, 16 * 3072);
  };
  const rsrc_t rw = mkrs(w >= 4 ? c.wu : c.wg, 640 * 3072);
  u32 xvo[2];
  sfor<2>([&](auto I) { constexpr int i = I; xvo[i] = (u32)((panel * 128 + 8 * (2 * w + i) + (l >> 3)) * 1280 + (((l & 7) ^ ((4 * i + q) & 7)) * 16)); });
  xvo[1] -= 1024;
  const u32 wvo = (u32)((8 * (w & 3) + q) * 3072 + grp * 256 + ((r ^ (4 * q)) * 16)), wvo1 = wvo + 4 * 3072 - 1024;
  const u32 m0b = w * 2048;
  auto issue = [&](auto Q) {
    constexpr int qq = Q;
    if constexpr (qIsX(qq)) DMA2<qSlotOff(qq), qIdx(qq) * 128>(m0b, xvo[0], xvo[1], c.rx);
    else DMA2<qSlotOff(qq), qIdx(qq) * 32 * 3072>(m0b, wvo, wvo1, rw);
  };
  __builtin_amdgcn_s_barrier();
  issue(IC(0));
  if (w < 4) d32_make(l, w, sl, false, P_D);
  const u32 xb = (w < 3 ? 32768 : 114688 - 3 * 5120) + w * 5120;
  u32 xw = xb + q * 1280 + r * 16, xr = xb + l * 8, xt = xb + 1024 + (l & 31) * 8, tw[4];
  sfor<4>([&](auto Z) { tw[Z] = P_T + (r >> 1) * 2048 + (r & 1) * 512 + (w >> 2) * 1024 + (q >> 1) * 256 + (((4 * (w & 3) + Z) ^ (r >> 1)) * 16) + (q & 1) * 8; OPAQUE(tw[Z]); });
  u32 tr = P_T + w * 2048 + ((l ^ w) * 16), va[2], v0 = P_V + (4 * w + q) * P_VP + r * 16;
  sfor<2>([&](auto U) { va[U] = w < 7 ? P_V + (16 * U + r) * P_VP + w * 64 + q * 16 : tr; OPAQUE(va[U]); });
  u32 z0 = WS_Z + sl * 1536 + sub * 512 + (4 * w + q) * 98304 + r * 16;
  u32 so = (r == 0 && sub == 0) ? WS_STAT + (64 * (4 * w + q) + sl) * 8 : OOB, k11 = 0x3f803f80u;
  float dc = (sub == 0 ? r == 0 : (sub == 2 && r == 12)) ? -40.0f : 0.f;
  OPAQUE(xw); OPAQUE(xr); OPAQUE(xt); OPAQUE(tr); OPAQUE(v0); OPAQUE(z0); OPAQUE(so); PIN(dc);
  asm volatile("" : "+s"(k11));
  const int sh = w & 1, ch = w >> 1;
  const int hh = l >> 5;
  u32 ta[3], xa[4];
  sfor<2>([&](auto Z) { ta[1 + Z] = (1 + Z) * 65536 + (u32)((r >> 2) * 256 + ((ch ^ (r >> 2)) * 64) + (q & 1) * 32 + (l & 3) * 8 + hh * 2048); OPAQUE(ta[1 + Z]); });
  sfor<4>([&](auto M) { xa[M] = (u32)((sh * 64 + (l & 31)) * 128 + (((2 * M + hh) ^ ((l >> 1) & 7)) * 16)); OPAQUE(xa[M]); });
  u32 cso = l < 8 ? WS_CS + ((cj / 24) * 3072 + (cc / 6) * 1536 + 384 * (c.xcd >> 1) + 64 * (cc % 6) + w * 8 + l) * 16 + ((cj / 12) & 1) * 8 : OOB;
  u32 fzo = threadIdx.x == 0 ? WS_FLAG + (F_Z + 64 * sub + sl) * 128 : OOB, fwo = w == 0 ? WS_FLAG + (F_W + l) * 128 : OOB;
  u32 sgo = WS_STAT + (panel * 128 + sh * 64 + l) * 8, cgo = WS_CS + (hh * 3072 + grp * 128 + ch * 32 + (l & 31)) * 16;
  u32 ho = WS_H + ((panel * 8 + sh * 4 + (q & 1)) * 48 + grp * 4 + ch) * 1024 + hh * 256 + r * 16;
  u32 csw = P_CS + w * 256 + (8 * (l & 7) + (l >> 3)) * 4, csr = P_CS + (l >> 3) * 256 + (w * 8 + (l & 7)) * 4;
  OPAQUE(csw); OPAQUE(csr); OPAQUE(cso); OPAQUE(fzo); OPAQUE(fwo); OPAQUE(sgo); OPAQUE(cgo); OPAQUE(ho);
  asm volatile("s_waitcnt vmcnt(6)" : "+v"(xv[0]), "+v"(xv[1]), "+v"(xv[2]), "+v"(xv[3]), "+v"(xv[4])::"memory");
  sfor<5>([&](auto I) { *(LAS v4u*)(xw + I * 256) = xv[I]; });
  issue(IC(1));
  bf16x8 x0[4], x1[4];
  sfor<4>([&](auto Z) { x0[Z] = cat(trr(xr + Z * 1280), trr(xr + Z * 1280 + 512)); const bf16x4 t = trr(xt + Z * 1280); x1[Z] = cat(t, t); });
  issue(IC(2));
  float sa[4] = {0.f, 0.f, 0.f, 0.f}, qa[4] = {0.f, 0.f, 0.f, 0.f};
  sfor<20>([&](auto E) { const u32 a = xv[E / 4][E % 4]; sa[E % 4] = dot2(k11, a, sa[E % 4]); qa[E % 4] = dot2(a, a, qa[E % 4]); });
  float s0 = (sa[0] + sa[1]) + (sa[2] + sa[3]), q0 = (qa[0] + qa[1]) + (qa[2] + qa[3]);
  GAP();
  issue(IC(3));
  asm volatile("s_waitcnt vmcnt(10)" : "+v"(sf[0][0]), "+v"(sf[0][1])::"memory");
  f32x4 t1[4];
  sfor<4>([&](auto Z) { t1[Z] = mfma16(x1[Z], asbf(sf[0][1]), mfma16(x0[Z], asbf(sf[0][0]), z4)); });
  s0 += dpp<0xB1>(s0); q0 += dpp<0xB1>(q0); s0 += dpp<0x4E>(s0); q0 += dpp<0x4E>(q0);
  s0 += dpp<0x141>(s0); q0 += dpp<0x141>(q0); s0 += dpp<0x140>(s0); q0 += dpp<0x140>(q0);
  const float mean = s0 * (1.0f / 640.0f), var = __builtin_fmaxf(__builtin_fmaf(q0, 1.0f / 640.0f, -mean * mean), 0.f);
  const u32 stat = cvtpk(mean, __builtin_amdgcn_sqrtf(rbf(rbf(var) + 1e-5f)));
  const float rinv = __builtin_amdgcn_rcpf(bfhi(stat)), mr = bflo(stat) * rinv;
  sfor<4>([&](auto Z) {
    const float ri = __uint_as_float(__builtin_amdgcn_readlane(__float_as_uint(rinv), 16 * Z));
    const float mz = __uint_as_float(__builtin_amdgcn_readlane(__float_as_uint(mr), 16 * Z)) * dc;
    *(LAS v2u*)tw[Z] = pack4(t1[Z] * ri + mz);
  });
  asm volatile("s_waitcnt vmcnt(8) lgkmcnt(0)\n\ts_barrier" : "+v"(mf[0]), "+v"(mf[1])::"memory");
  tag_wait(c, raw, epoch);
  {
    const bf16x8 tt0 = *(LAS bf16x8*)tr, tt1 = *(LAS bf16x8*)(tr + 1024);
    D32 d;
    d32_read(d, l, P_D);
    cs_issue();
    const bf16x8 zr = pack44(mfma16(tt0, asbf(mf[0]), z4), mfma16(tt1, asbf(mf[0]), z4)), zi = pack44(mfma16(tt0, asbf(mf[1]), z4), mfma16(tt1, asbf(mf[1]), z4));
    sfor<2>([&](auto U) { *(LAS bf16x8*)va[U] = pack44(mfma16(zi, d.s[U], mfma16(zr, d.c[U], z4)), mfma16(zr, d.n[U], mfma16(zi, d.c[U], z4))); });
  }
  issue(IC(4));
  WAITBAR<63>();
  st128<SC1>(*(LAS v4u*)v0, c.rws, z0);
  st128<SC1>(*(LAS v4u*)(v0 + 256), c.rws, z0 + 256);
  st64<SC1>(v2u{stat, c.tag}, c.rws, so);
  CFENCE();
  if (w >= 4) __builtin_amdgcn_s_setprio(1);
  f32x16 acc[2][2];
  sfor<64>([&](auto E) { acc[E >> 5][(E >> 4) & 1][E & 15] = 0.f; });
  bf16x8 wf[2][2][2], xf[2][2][2];
  auto rdw = [&](auto ST, auto H, auto T) {
    constexpr int st = ST, off = (NXS + st % NWS) * SLOTB + T * 8192 + H * 4096;
    wf[st & 1][H][T] = cat(trr(ta[off >> 16] + (off & 65535)), trr(ta[off >> 16] + (off & 65535) + 1024));
  };
  auto rdx = [&](auto ST, auto H, auto I) {
    constexpr int st = ST;
    xf[st & 1][H][I] = *(LAS bf16x8*)(xa[2 * (st & 1) + H] + ((st >> 1) % NXS) * SLOTB + I * 4096);
  };
  WAITBAR<waitN(0)>();
  sfor<4>([&](auto I) { rdw(IC(0), IC(I >> 1), IC(I & 1)); rdx(IC(0), IC(I >> 1), IC(I & 1)); });
  GAP();
  bf16x8 cf[2], nm[2];
  v2u sg;
  v4u cg[2];
  float ct[8], r2[2];
  u32 fw;
  auto side_loads = [&]() { sg = ld64<SC1>(c.rws, sgo); cg[0] = ld128<SC1>(c.rws, cgo); cg[1] = ld128<SC1>(c.rws, cgo, 1536 * 16); };
  auto side_ok = [&]() { return __all(sg.y == c.tag && cg[0].y == c.tag && cg[0].w == c.tag && cg[1].y == c.tag && cg[1].w == c.tag); };
  sfor<20>([&](auto ST) {
    constexpr int st = ST;
    if constexpr (st == ST_CS) WAITBAR<waitN(st)>();
    else if constexpr (st == 19) WAITBAR<63>();
    else if constexpr (st > 0) WAITBARV<waitN(st)>();
    if constexpr (st == WD1 - 1) {
      OPAQUE(fw);
      if (UNLIKELY(w == 0 && !__all(fw == c.tag))) poll_flags(c, F_W + l);
      GAP();
    }
    if constexpr (st == 18) {
      if (UNLIKELY(!side_ok())) {
#pragma nounroll
        for (u32 n = 0; n < SPIN_CAP; ++n) {
          __builtin_amdgcn_s_sleep(16);
          side_loads();
          if (side_ok()) break;
        }
      }
      CFENCE();
      GAP();
    }
    sfor<8>([&](auto G) {
      constexpr int g = G, h = g >> 2, t = (g >> 1) & 1, i = g & 1;
      acc[t][i] = mfma32(wf[st & 1][h][t], xf[st & 1][h][i], acc[t][i]);
      if constexpr (st + 1 < 20) {
        if constexpr (i == 1) rdw(IC(st + 1), IC(h), IC(t));
        else rdx(IC(st + 1), IC(h), IC(t));
      }
      if constexpr (dmaAt(st, g) >= 0) issue(IC(dmaAt(st, g)));
      if constexpr (wdAt(st, g) >= 0) wd_load<wdAt(st, g)>(c, wd);
      if constexpr (st == 1 && g == 1) st32<SC1>(c.tag, c.rws, fzo);
      if constexpr (st == ST_FW && g == 2) BLD32(fw, fwo, c.rws);
      if constexpr (st == 2 && g == 0) { PIN(cv[0]); PIN(cv[1]); PIN(cv[2]); }
      if constexpr ((st == 2 || st == 3) && i == 1) {
        constexpr int e = 4 * (st - 2) + (g >> 1);
        ct[e] = (e & 1) ? bfhi(cv[0][e / 2]) + bfhi(cv[1][e / 2]) + bfhi(cv[2][e / 2]) : bflo(cv[0][e / 2]) + bflo(cv[1][e / 2]) + bflo(cv[2][e / 2]);
        PIN(ct[e]);
      }
      if constexpr (st == ST_CS - 1) {
        if constexpr (g == 1 || g == 2) { ct[g - 1] = fold32(ct[g - 1], ct[g + 3]); ct[g + 1] = fold32(ct[g + 1], ct[g + 5]); }
        if constexpr (g == 3) { ct[0] = fold16(ct[0], ct[2]); ct[1] = fold16(ct[1], ct[3]); }
        if constexpr (g == 5) {
          const bool up = l & 8;
          *(LAS float*)csw = (up ? ct[1] : ct[0]) + dpp<0x128>(up ? ct[0] : ct[1]);
        }
      }
      if constexpr (st == ST_CS) {
        if constexpr (g == 0) ct[0] = *(LAS float*)csr;
        if constexpr (g == 2) ct[0] += dpp<0x128>(ct[0]);
        if constexpr (g == 3) ct[0] = fold16(ct[0], ct[0]);
        if constexpr (g == 5) ct[0] = fold32(ct[0], ct[0]);
        if constexpr (g == 6) st64<SC1>(v2u{cvtpk(ct[0], ct[0] - rbf(ct[0])), c.tag}, c.rws, cso);
      }
      if constexpr (st == ST_SIDE && g == 0) side_loads();
      if constexpr (st == 18 && g == 1) {
        const auto p = __builtin_amdgcn_permlane32_swap(sg.x, sg.x, false, false);
        sg = v2u{p[0], p[1]};
        cf[0] = asbf(v4u{cg[0].x, cg[0].z, 0u, 0u}); cf[1] = asbf(v4u{cg[1].x, cg[1].z, 0u, 0u});
      }
      if constexpr (st == 18 && (g == 3 || g == 5)) {
        constexpr int j = (g - 3) / 2;
        const u32 m1 = __builtin_amdgcn_perm(sg[j], sg[j], 0x01000100u) ^ 0x80008000u;
        nm[j] = asbf(v4u{m1, m1, 0u, 0u});
        const float ri = __builtin_amdgcn_rcpf(bfhi(sg[j]));
        r2[j] = ri * ri;
      }
      if constexpr (st == 19 && g < 4) acc[t][i] = mfma32(cf[t], nm[i], acc[t][i]);
      GAP();
    });
  });
  sfor<2>([&](auto I) {
    const f32x16 h = acc[0][I] * acc[1][I] * r2[I];
    sfor<2>([&](auto G) {
      st128<SC1>(v4u{cvtpk(h[8 * G], h[8 * G + 1]), cvtpk(h[8 * G + 2], h[8 * G + 3]), cvtpk(h[8 * G + 4], h[8 * G + 5]), cvtpk(h[8 * G + 6], h[8 * G + 7])}, c.rws,
                 ho + I * 98304 + G * 512);
    });
  });
  __builtin_amdgcn_s_setprio(0);
  CFENCE();
  OutAddr<NPG> oad;
  out_prep(c, oad);
  arrive_publish(c, F_H + 12 * panel + grp);
  const u32 fh = ld32<SC1>(c.rws, WS_FLAG + oad.ho * 128);
  CFENCE();
  sfor<30 - WDL>([&](auto I) { wd_load<WDL + I>(c, wd); });
  CFENCE();
  out_phase(c, c.out, wd, oad, __all(fh == c.tag));
  if (threadIdx.x == 0 && blockIdx.x == 0) st32<SC1>(epoch, c.rws, 0);
}

constexpr u32 S_W = 0, S_WQ = 3088, S_BP = 784, S_CP = 1568, S_U = 50176, S_UP = 1352;
constexpr u32 S_FI = 99328, S_DI = S_FI + 14336, S_MI = S_DI + 4096, S_SCR = S_MI;
static_assert(32 * S_WQ <= S_FI && 32 * S_CP <= S_U && S_U + 32 * S_UP <= S_FI, "");
template <int NS> DI void p3_groups(const D32& d, const v4u (&mf)[3][2], const u32 (&va)[3], const u32 (&ua)[3]) {
  const f32x4 z4 = {0.f, 0.f, 0.f, 0.f};
  bf16x8 vr[NS], vi[NS];
  sfor<NS>([&](auto S) { vr[S] = cat(trr(va[S]), trr(va[S] + 16 * S_CP)); vi[S] = cat(trr(va[S] + 8), trr(va[S] + 16 * S_CP + 8)); });
  sfor<NS>([&](auto S) {
    sfor<2>([&](auto I) {
      const bf16x8 y = pack44(mfma16(vi[S], d.n[I], mfma16(vr[S], d.c[I], z4)), mfma16(vr[S], d.s[I], mfma16(vi[S], d.c[I], z4)));
      sfor<2>([&](auto C) { *(LAS v2u*)(ua[S] + I * 16 * S_UP + C * 32) = pack4(mfma16(asbf(mf[S][C]), y, z4)); });
    });
  });
}
struct F64 { bf16x8 c[2][2], s[2][2], n[2][2]; };
template <int NU> DI void p2_units(const F64& f, const f32x4 (&wr)[2][2][2], const f32x4 (&wi)[2][2][2], u32 ra, u32 wa) {
  const f32x4 z4 = {0.f, 0.f, 0.f, 0.f};
  bf16x8 vr[NU][2], vi[NU][2], pr[NU][2], pi[NU][2];
  sfor<2 * NU>([&](auto J) {
    constexpr int S = J >> 1, P = J & 1;
    const u32 ad = ra + S * 576 + P * S_BP;
    vr[S][P] = cat(trr(ad), trr(ad + 32 * S_BP)); vi[S][P] = cat(trr(ad + 8), trr(ad + 32 * S_BP + 8));
  });
  CFENCE();
  sfor<2 * NU>([&](auto J) {
    constexpr int S = J >> 1, P = J & 1;
    const f32x4 er = mfma16(f.s[P][0], vi[S][0], mfma16(f.c[P][0], vr[S][0], z4)), ei = mfma16(f.n[P][0], vr[S][0], mfma16(f.c[P][0], vi[S][0], z4));
    const f32x4 od = mfma16(f.s[P][1], vi[S][1], mfma16(f.c[P][1], vr[S][1], z4)), oi = mfma16(f.n[P][1], vr[S][1], mfma16(f.c[P][1], vi[S][1], z4));
    const f32x4 ar = er + od, ai = ei + oi, br = er - od, bi = ei - oi;
    pr[S][P] = pack44(ar * wr[S][P][0] - ai * wi[S][P][0], br * wr[S][P][1] - bi * wi[S][P][1]);
    pi[S][P] = pack44(ar * wi[S][P][0] + ai * wr[S][P][0], br * wi[S][P][1] + bi * wr[S][P][1]);
  });
  sfor<2 * NU>([&](auto J) {
    constexpr int S = J >> 1, P = J & 1;
    const f32x4 er = mfma16(pi[S][0], f.n[P][0], mfma16(pr[S][0], f.c[P][0], z4)), ei = mfma16(pr[S][0], f.s[P][0], mfma16(pi[S][0], f.c[P][0], z4));
    const u32 ad = wa + S * 576 + P * S_BP;
    const f32x4 od = mfma16(pi[S][1], f.n[P][1], mfma16(pr[S][1], f.c[P][1], z4)), oi = mfma16(pr[S][1], f.s[P][1], mfma16(pi[S][1], f.c[P][1], z4));
    *(LAS bf16x8*)ad = pack44(er + od, ei + oi);
    *(LAS bf16x8*)(ad + 32 * S_BP) = pack44(er - od, ei - oi);
  });
}

template <int OFF> DI void gather4(f32x4& v, u32 a) {
  v = f32x4{bflo(*(LAS u16*)(a + OFF)), bflo(*(LAS u16*)(a + OFF + 512)), bflo(*(LAS u16*)(a + OFF + S_WQ)), bflo(*(LAS u16*)(a + OFF + S_WQ + 512))};
}
DI void spc_phase(Ctx& c, u32 raw) {
  u32 epoch;
  v4u wd[30];
  OutAddr<16 - 3 * NPG> oad;
  const int l = c.l, w = c.w, q = l >> 4, r = l & 15, rr = l >> 2;
  const int si = c.xcd * 8 + (c.idx & 7), hf = si & 1;
  const rsrc_t rt = mkrs(&TABLES, T_END);
  const int it = si * 8 + w;
  v4u dv[4];
  {
    const u32 vo = it < 480 ? (u32)(((it / 10) * 32 + (l >> 3)) * 1280 + (it % 10) * 128 + (l & 7) * 16) : OOB;
    sfor<4>([&](auto I) { BLD(dv[I], vo, mkrs(c.wd, 1536 * 1280), ((I & 1) * 16 + (I >> 1) * 8) * 1280); });
  }
  const rsrc_t rf = mkrs(w < 4 ? c.wr : c.wi, 2048 * 642);
  const u32 fo = ((((si >> 1) + 32 * (16 * (w & 3) + q)) * 642) & ~15u) + r * 16, fo2 = r < 9 ? fo : OOB;
  auto f_load = [&](auto J) {
    const u32 p = (J / 3) ^ (2 * hf);
    DMA(S_W + (4 * w + p) * S_WQ + (p >> 1) * 4 + (J % 3) * 1024, J % 3 < 2 ? fo : fo2, rf, p * 128 * 642 + (J % 3) * 256);
  };
  __builtin_amdgcn_s_barrier();
  sfor<3>([&](auto J) { f_load(J); });
  if (w < 4) d32_make(l, w, si, true, S_DI);
  asm volatile("s_waitcnt vmcnt(3)" : "+v"(dv[0]), "+v"(dv[1]), "+v"(dv[2]), "+v"(dv[3])::"memory");
  sfor<3>([&](auto J) { f_load(IC(J + 3)); });
  {
    v4u gd[4];
    tile_tr(c, dv, gd, S_SCR + w * 2048);
    sfor<4>([&](auto J) {
      const int c16 = (it % 10) * 4 + J;
      st128<SC1>(gd[J], c.rws, it < 480 ? WS_WD + (((c16 / 5) * 48 + it / 10) * 5 + c16 % 5) * 1024 + l * 16 : OOB);
    });
  }
  sfor<6>([&](auto J) { f_load(IC(J + 6)); });
  DMA(S_FI + w * 1024, l * 16, rt, T_F - 6144 + w * 1024);
  if (w < 6) DMA(S_FI + 8192 + w * 1024, l * 16, rt, T_F + 2048 + w * 1024);
  const bool two = w < 3 - hf;
  f32x4 wr[2][2][2], wi[2][2][2];
  u32 ga0[2], ga1[2];
  sfor<2>([&](auto S) {
    const u32 by = 2 * col_k(11 * hf + w + 8 * S, r) + (si & 14);
    ga0[S] = S_W + 2 * q * S_WQ + 4 * (q & 1) + (by >> 8) * 1024 + (by & 255); ga1[S] = ga0[S] + 16 * S_WQ;
    OPAQUE(ga0[S]); OPAQUE(ga1[S]);
  });
  WAITBAR<0>();
  tag_wait(c, raw, epoch);
  if (w == 6) raise(c, F_W + si);
  sfor<6>([&](auto J) { if (J < 4 || w < 5) DMA(S_MI + (w + 8 * (J >> 1)) * 2048 + (J & 1) * 1024, l * 16, rt, (w + 8 * (J >> 1)) * 4096 + (2 + (J & 1)) * 1024); });
  sfor<2>([&](auto S) {
    if (S == 0 || two) {
      sfor<4>([&](auto J) {
        constexpr int p = J >> 1, b = J & 1;
        gather4<8 * b * S_WQ + p * 256>(wr[S][p][b], ga0[S]);
        gather4<8 * b * S_WQ + p * 256>(wi[S][p][b], ga1[S]);
      });
      sfor<4>([&](auto E) { PIN(wr[S][E >> 1][E & 1]); PIN(wi[S][E >> 1][E & 1]); });
    }
  });
  const u32 zf = (l & 16) ? F_W + 8 * w + (l & 7) : F_Z + 64 * (hf + ((l >> 3) & 1)) + 8 * w + (l & 7);
  const u32 fz = ld32<SC1>(c.rws, WS_FLAG + zf * 128);
  CFENCE();
  F64 f;
  sfor<4>([&](auto P) {
    f.c[P >> 1][P & 1] = *(LAS bf16x8*)(S_FI + 6144 + P * 1024 + l * 16);
    f.s[P >> 1][P & 1] = *(LAS bf16x8*)(S_FI + 10240 + P * 1024 + l * 16);
    f.n[P >> 1][P & 1] = neg8(f.s[P >> 1][P & 1]);
    PIN(f.n[P >> 1][P & 1]);
  });
  const int sl0 = w + (w >= (hf ? 3 : 7));
  u32 bb = S_W + (8 * w + q) * S_BP + r * 16, ra = S_W + sl0 * 64 + 2 * rr * S_BP + (l & 3) * 16, wa = S_W + sl0 * 64 + 2 * r * S_BP + q * 16;
  u32 zi = WS_Z + (si >> 1) * 98304 + hf * 768 + (8 * w + q) * 1536 + r * 16;
  u32 yo = WS_Y + (si >> 1) * 1536 + hf * 768 + (8 * w + q) * 49152 + r * 16;
  OPAQUE(bb); OPAQUE(ra); OPAQUE(wa); OPAQUE(zi); OPAQUE(yo);
  WAITBAR<63>();
  if (!__all(fz == c.tag)) poll_flags(c, zf);
  CFENCE();
  v4u sf[3][2], mf[3][2];
  {
    v4u y[6];
    sfor<6>([&](auto I) { y[I] = ld128<SC1>(c.rws, zi + (I % 3) * 256, (I / 3) * 4 * 1536); });
    GAP();
    sfor<6>([&](auto I) { *(LAS v4u*)(bb + (I / 3) * 4 * S_BP + (I % 3) * 256) = y[I]; });
  }
  WAITBAR<63>();
  if (two) p2_units<2>(f, wr, wi, ra, wa);
  else {
    sfor<14>([&](auto I) { wd_load<I>(c, wd); });
    CFENCE();
    p2_units<1>(f, wr, wi, ra, wa);
  }
  WAITBAR<63>();
  sfor<6>([&](auto I) { st128<SC1>(*(LAS v4u*)(bb + (I / 3) * 4 * S_BP + (I % 3) * 256), c.rws, yo + (I % 3) * 256 + (I / 3) * 4 * 49152); });
  CFENCE();
  sfor<6>([&](auto J) {
    mf[J >> 1][J & 1] = *(LAS v4u*)(S_MI + ((J < 4 || w < 5) ? w + 8 * (J >> 1) : 0) * 2048 + (J & 1) * 1024 + l * 16);
    sf[J >> 1][J & 1] = *(LAS v4u*)(S_FI + J * 1024 + l * 16);
  });
  D32 d;
  d32_read(d, l, S_DI);
  PIN(d.n[0]); PIN(d.n[1]);
  u32 yi = WS_Y + si * 49152 + (4 * w + q) * 1536 + r * 16, cw = S_W + (4 * w + q) * S_CP + r * 16;
  u32 ur = S_U + 4 * w * S_UP + l * 8, ut = S_U + 4 * w * S_UP + 1024 + (l < 40 ? l : (l & 7) + 32) * 8;
  u32 ao = WS_A + (64 * (4 * w + (q & 1)) + si) * 1280 + r * 32 + (q >> 1) * 16, ao2 = r < 8 ? ao : OOB, ga[3], gb[3];
  sfor<3>([&](auto S) { const int g = w + 8 * S; ga[S] = S_W + rr * S_CP + (g + g / 7) * 64 + (l & 3) * 16; gb[S] = S_U + r * S_UP + g * 64 + q * 8; OPAQUE(ga[S]); OPAQUE(gb[S]); });
  OPAQUE(yi); OPAQUE(cw); OPAQUE(ur); OPAQUE(ut); OPAQUE(ao); OPAQUE(ao2);
  DRAIN();
  raise(c, F_Y + 64 * w + si);
  WAITBAR<63>();
  out_prep(c, oad);
  poll_flags(c, F_Y + 64 * (si >> 3) + 8 * w + (l & 7));
  {
    v4u y[6];
    sfor<6>([&](auto I) { y[I] = ld128<SC1>(c.rws, yi + I * 256); });
    GAP();
    sfor<6>([&](auto I) { *(LAS v4u*)(cw + I * 256) = y[I]; });
  }
  WAITBAR<63>();
  if (w < 5) p3_groups<3>(d, mf, ga, gb);
  else p3_groups<2>(d, mf, ga, gb);
  CFENCE();
  const u32 fh = ld32<SC1>(c.rws, WS_FLAG + oad.ho * 128);
  WAITBAR<63>();
  sfor<2>([&](auto Z) {
    f32x4 o[2][3];
    sfor<2>([&](auto Y) { stage40<3>(ur + (2 * Z + Y) * S_UP, ut + (2 * Z + Y) * S_UP, sf, o[Y]); });
    sfor<3>([&](auto T) {
      const v2u a = pack4(o[0][T]), b = pack4(o[1][T]);
      const auto x = __builtin_amdgcn_permlane16_swap(a.x, b.x, false, false), y = __builtin_amdgcn_permlane16_swap(a.y, b.y, false, false);
      st128<SC1>(v4u{x[0], y[0], x[1], y[1]}, c.rws, (T < 2 ? ao : ao2) + T * 512 + Z * 128 * 1280);
    });
  });
  WAITBAR<0>();
  if (w == 6) raise(c, F_A + si);
  if (two) sfor<14>([&](auto I) { wd_load<I>(c, wd); });
  sfor<16>([&](auto I) { wd_load<14 + I>(c, wd); });
  out_phase(c, c.out, wd, oad, __all(fh == c.tag));
}

__global__ __launch_bounds__(512, 2) void k_all(const u16* x, const u16* wr, const u16* wi, const u16* wg, const u16* wu, const u16* wd, u16* out, char* ws) {
  Ctx c;
  c.rx = mkrs(x, 2048 * 1280);
  c.rws = mkrs(ws, WS_END);
  c.wr = wr; c.wi = wi; c.wg = wg; c.wu = wu; c.wd = wd; c.out = out;
  c.l = threadIdx.x & 63;
  c.w = __builtin_amdgcn_readfirstlane(threadIdx.x >> 6);
  c.xcd = blockIdx.x & 7;
  c.idx = blockIdx.x >> 3;
  u32 raw;
  asm volatile("s_load_dword %0, %1, 0x0" : "=s"(raw) : "s"(ws) : "memory");
  if (threadIdx.x == 0) *(LAS u32*)L_CTL = 0;
  const char* pc;
  asm volatile("s_getpc_b64 %0" : "=s"(pc));
  if ((c.idx >> 3) == 3 && c.w >= 4) {
    const u32 j = (c.idx & 3) * 64 + c.l;
    const u32 jt = ((c.idx & 7) * 2 + (c.w & 1)) * 64 + c.l;
    const char* p = c.w < 6 ? (const char*)&TABLES + (jt < T_END / 128 ? jt : 0) * 128 : c.w == 7 ? pc + (j < CODE_LINES ? j : 0) * 128 : (c.l < 24 ? ws + c.l * 0x100000 + 4096 : (const char*)out + (c.l & 1) * 0x200000);
    asm volatile("s_mov_b32 m0, %1\n\ts_nop 0\n\tglobal_load_lds_dword %0, off sc1" ::"v"(p), "s"(150528u) : "memory");
  }
  const bool spc = (c.idx >> 3) == 3;
  if (UNLIKELY(spc)) spc_phase(c, raw);
  else glu_phase(c, raw);
}

extern "C" void kernel_launch(void* const* d_in, const int* in_sizes, int n_in, void* d_out, int out_size, void* d_ws, size_t ws_size, hipStream_t stream) {
  (void)hipFuncSetAttribute((const void*)k_all, hipFuncAttributeMaxDynamicSharedMemorySize, LDS_BYTES);
  k_all<<<256, 512, LDS_BYTES, stream>>>((const u16*)d_in[0], (const u16*)d_in[1], (const u16*)d_in[2], (const u16*)d_in[4], (const u16*)d_in[5],
                                         (const u16*)d_in[6], (u16*)d_out, (char*)d_ws);
}
```
